```python
import torch

def _softmax(x, axis=-1):
    return torch.softmax(x, dim=axis)
def _gelu(x):
    return torch.nn.functional.gelu(x, approximate="tanh")
def _silu(x):
    return torch.nn.functional.silu(x)
def _sigmoid(x):
    return torch.sigmoid(x)
def _layernorm(x, axis=-1, eps=1e-5):
    m = x.mean(dim=axis, keepdim=True)
    v = x.var(dim=axis, keepdim=True, unbiased=False)
    return (x - m) / torch.sqrt(v + eps)
def _scatter_add(tgt, idx, src, axis=0):
    return tgt.clone().index_add_(axis, idx, src)
def _segment_sum(src, seg, n):
    out = torch.zeros((n,) + src.shape[1:], dtype=src.dtype)
    return out.index_add_(0, seg, src)
def _pad(x, pw):
    flat = tuple(v for pair in reversed(pw) for v in pair)
    return torch.nn.functional.pad(x, flat)

kernel_name = 'gated_mix__ssm_conv_s4d__dense_activation_mish__prompt'

def setup_inputs():

    _w = (lambda a: (lambda t: t.to(torch.bfloat16) if t.is_floating_point() else t)(torch.as_tensor(a)))
    def _A_setup():
        Bsz, L, D, N = 1, 2048, 576, 128
        return {
            "u": _w((lambda *s: torch.randn(*s, dtype=torch.bfloat16) * 0.05)(Bsz, L, D)),
            "log_A_real": _w((lambda *s: torch.randn(*s, dtype=torch.bfloat16) * 6.4)(D, N) * 0.5),
            "A_imag": _w((lambda *s: torch.randn(*s, dtype=torch.bfloat16) * 0.05)(D, N) * 3.14),
            "C_real": _w((lambda *s: torch.randn(*s, dtype=torch.bfloat16) * 0.1)(D, N) / N ** 0.5),
            "C_imag": _w((lambda *s: torch.randn(*s, dtype=torch.bfloat16) * 0.1)(D, N) / N ** 0.5),
            "log_dt": _w((lambda *s: torch.randn(*s, dtype=torch.bfloat16) * 0.05)(D) * 0.5 - 4.0),
            "D_skip": _w((lambda *s: torch.randn(*s, dtype=torch.bfloat16) * 0.05)(D)),
        }
    _A_in = _A_setup()

    def _B_setup():
        B, S, D, F = 1, 2048, 576, 4608
        return {
            "x": _w((lambda *s: torch.randn(*s, dtype=torch.bfloat16) * 0.05)(B, S, D)),
            "w1": _w((lambda *s: torch.randn(*s, dtype=torch.bfloat16) * 6.4)(D, F) * (D ** -0.5)),
            "b1": _w((lambda *s: torch.randn(*s, dtype=torch.bfloat16) * 0.05)(F)),
            "w2": _w((lambda *s: torch.randn(*s, dtype=torch.bfloat16) * 0.05)(F, D) * (F ** -0.5)),
        }
    _B_in = _B_setup()

    return {
        "A_u": _A_in["u"],
        "A_log_A_real": _A_in["log_A_real"],
        "A_A_imag": _A_in["A_imag"],
        "A_C_real": _A_in["C_real"],
        "A_C_imag": _A_in["C_imag"],
        "A_log_dt": _A_in["log_dt"],
        "A_D_skip": _A_in["D_skip"],
        "B_x": _B_in["x"],
        "B_w1": _B_in["w1"],
        "B_b1": _B_in["b1"],
        "B_w2": _B_in["w2"],
        "gate_w": _w((lambda *s: torch.randn(*s, dtype=torch.bfloat16) * 0.05)(576) * 16.0),
    }

def reference(A_u, A_log_A_real, A_A_imag, A_C_real, A_C_imag, A_log_dt, A_D_skip, B_x, B_w1, B_b1, B_w2, gate_w):
    def _A_ref(u, log_A_real, A_imag, C_real, C_imag, log_dt, D_skip):
        L = u.shape[1]
        dt = torch.exp(log_dt)
        A = -torch.exp(log_A_real) + 1j * A_imag
        C = (C_real + 1j * C_imag) * dt[:, None]
        pos = (lambda x, dt: x.to(dt))(torch.arange(L), log_dt.dtype)
        P = torch.exp((A * dt[:, None])[:, :, None] * pos[None, None, :])
        K = 2.0 * torch.einsum("dn,dnl->dl", C, P).real
        L2 = 2 * L
        u_t = (lambda x, *d: x.permute(*d))(u, 0, 2, 1)
        Kf = (lambda x, *a, **k: torch.fft.rfft(x.float(), *a, **k))(K, L2)
        Uf = (lambda x, *a, **k: torch.fft.rfft(x.float(), *a, **k))(u_t, L2)
        y = (lambda x, *a, **k: torch.fft.irfft(x, *a, **k).to(torch.bfloat16))(Uf * Kf[None, :, :], L2)[:, :, :L]
        y = (lambda x, *d: x.permute(*d))(y, 0, 2, 1)
        return y + u * D_skip[None, None, :]

    def _B_ref(x, w1, b1, w2):
        h = torch.einsum("bsd,df->bsf", x, w1) + b1
        sp = torch.log1p(torch.exp(-torch.abs(h))) + torch.where(h > 0, h, torch.zeros_like(h))
        act = h * torch.tanh(sp)
        return torch.einsum("bsf,fd->bsd", act, w2)

    x = A_u
    g = _sigmoid((x * gate_w).sum(-1, keepdims=True))
    a = _A_ref(x, A_log_A_real, A_A_imag, A_C_real, A_C_imag, A_log_dt, A_D_skip)
    b = _B_ref(x, B_w1, B_b1, B_w2)
    return g * a + (1 - g) * b
```

```cpp
#include <hip/hip_runtime.h>
#include <hip/hip_bf16.h>
#include <stdint.h>
typedef unsigned short u16;
constexpr int L=2048, D=576, N=128, F=4608;
__device__ __forceinline__ float bf2f(u16 v){ return __uint_as_float(((unsigned)v)<<16); }
__device__ __forceinline__ u16 f2bf(float f){ unsigned u=__float_as_uint(f); u += 0x7fffu + ((u>>16)&1u); return (u16)(u>>16); }
__device__ __forceinline__ float rbf(float f){ return bf2f(f2bf(f)); }

__global__ void k_kgen(const u16* logA,const u16* Aim,const u16* Cre,const u16* Cim,const u16* logdt,float* K){
  int d=blockIdx.x; __shared__ float zr[N],zi[N],cr[N],ci[N];
  float dt=rbf(expf(bf2f(logdt[d])));
  if(threadIdx.x<N){int n=threadIdx.x; float a=rbf(expf(bf2f(logA[d*N+n]))); zr[n]=-a*dt; zi[n]=bf2f(Aim[d*N+n])*dt; cr[n]=bf2f(Cre[d*N+n])*dt; ci[n]=bf2f(Cim[d*N+n])*dt;}
  __syncthreads();
  for(int l=threadIdx.x;l<L;l+=blockDim.x){ float p=rbf((float)l); float s=0;
    for(int n=0;n<N;n++){ float m=expf(zr[n]*p); float sn,cs; sincosf(zi[n]*p,&sn,&cs); s+= m*(cr[n]*cs-ci[n]*sn);} K[d*L+l]=2.f*s; }
}
__global__ void k_conv(const u16* u,const float* K,float* y){
  int d=blockIdx.x; __shared__ float su[L], sk[L];
  for(int l=threadIdx.x;l<L;l+=blockDim.x){ su[l]=bf2f(u[l*D+d]); sk[l]=K[d*L+l]; }
  __syncthreads();
  for(int l=threadIdx.x;l<L;l+=blockDim.x){ float s=0; for(int j=0;j<=l;j++) s+=sk[j]*su[l-j]; y[l*D+d]=s; }
}
__global__ void k_g1(const u16* x,const u16* w1,const u16* b1,u16* act){
  int f=blockIdx.x*256+threadIdx.x; int r0=blockIdx.y*8; __shared__ float sx[8][D];
  for(int i=threadIdx.x;i<8*D;i+=256) sx[i/D][i%D]=bf2f(x[r0*D+i]);
  __syncthreads();
  float a[8]={0,0,0,0,0,0,0,0};
  for(int k=0;k<D;k++){ float w=bf2f(w1[k*F+f]); for(int r=0;r<8;r++) a[r]+=sx[r][k]*w; }
  float b=bf2f(b1[f]);
  for(int r=0;r<8;r++){ float h=a[r]+b; float e=__expf(h); float dd=fmaf(e,fmaf(e,0.5f,1.f),1.f); float m=h-h/dd; act[(size_t)(r0+r)*F+f]=f2bf(m);}
}
__global__ void k_g2(const u16* x,const u16* act,const u16* w2,const float* y,const u16* Dsk,const u16* gw,u16* out){
  int r0=blockIdx.x*4; int d=threadIdx.x; __shared__ float sa[4][F]; __shared__ float red[4][D];
  for(int i=threadIdx.x;i<4*F;i+=D) sa[i/F][i%F]=bf2f(act[(size_t)r0*F+i]);
  for(int r=0;r<4;r++) red[r][d]=bf2f(x[(r0+r)*D+d])*bf2f(gw[d]);
  __syncthreads();
  float a[4]={0,0,0,0};
  for(int k=0;k<F;k++){ float w=bf2f(w2[k*D+d]); for(int r=0;r<4;r++) a[r]+=sa[r][k]*w; }
  for(int r=0;r<4;r++){ float s=0; for(int k=0;k<D;k++) s+=red[r][k]; float g=1.f/(1.f+expf(-s));
    float av=y[(r0+r)*D+d]+bf2f(x[(r0+r)*D+d])*bf2f(Dsk[d]); out[(r0+r)*D+d]=f2bf(g*av+(1.f-g)*a[r]); }
}
extern "C" void kernel_launch(void* const* d_in,const int* in_sizes,int n_in,void* d_out,int out_size,void* d_ws,size_t ws_size,hipStream_t stream){
  const u16* u=(const u16*)d_in[0];
  float* K=(float*)d_ws; float* y=K+D*L; u16* act=(u16*)(y+L*D);
  k_kgen<<<D,256,0,stream>>>((const u16*)d_in[1],(const u16*)d_in[2],(const u16*)d_in[3],(const u16*)d_in[4],(const u16*)d_in[5],K);
  k_conv<<<D,256,0,stream>>>(u,K,y);
  k_g1<<<dim3(F/256,L/8),256,0,stream>>>(u,(const u16*)d_in[8],(const u16*)d_in[9],act);
  k_g2<<<L/4,D,0,stream>>>(u,act,(const u16*)d_in[10],y,(const u16*)d_in[6],(const u16*)d_in[11],(u16*)d_out);
}
```

```cpp
#include <hip/hip_runtime.h>
#include <hip/hip_bf16.h>
#include <stdint.h>
typedef unsigned short u16;
typedef unsigned long long u64;
typedef unsigned u32x4 __attribute__((ext_vector_type(4)));
typedef unsigned u32x2 __attribute__((ext_vector_type(2)));
typedef float f32x4 __attribute__((ext_vector_type(4)));
typedef float f32x2 __attribute__((ext_vector_type(2)));
typedef __bf16 bf16x2 __attribute__((ext_vector_type(2)));
#define BLKF 0
#define DIAGW 0
#define DI __device__ __forceinline__
constexpr int L=2048, D=576, N=128, F=4608;
DI float bf2f(u16 v){ return __uint_as_float(((unsigned)v)<<16); }
DI float blo(unsigned v){ return __uint_as_float(v<<16); }
DI float bhi(unsigned v){ return __uint_as_float(v&0xffff0000u); }
DI unsigned cvtpk(float lo,float hi){ f32x2 v={lo,hi}; bf16x2 b=__builtin_convertvector(v,bf16x2); return __builtin_bit_cast(unsigned,b); }
DI float rbf(float f){ return blo(cvtpk(f,0.f)); }
DI unsigned rfl(unsigned x){ return __builtin_amdgcn_readfirstlane(x); }

#define K1_CLOB "v18","v19","v20","v21","v22","v23","v24","v25","v26","v27","v28","v29","v30","v31","v32","v33","v34","v35","v36","v37","v38","v39","v40","v41","v42","v43","v44","v45","v46","v47","v48","v49","v50","v51","v52","v53","v54","v55","v56","v57","v58","v59","v60","v61","v62","v63","v64","v65","v66","v67","v68","v69","v70","v71","v72","v73","v74","v75","v76","v77","v78","v79","v80","v81","v82","v83","v84","v85","v86","v87","v88","v89","v90","v91","v92","v93","v94","v95","v96","v97","v98","v99","v100","v101","v102","v103","v104","v105","v106","v107","v108","v109","v110","v111","v112","v113","v114","v115","v116","v117","v118","v119","v120","v121","v122","v123","v124","v125","v126","v127","v128","v129","v130","v131","v132","v133","v134","v135","v136","v137","v138","v139","v140","v141","v142","v143","v144","v145","v146","v147","v148","v149","v150","v151","v152","v153","v154","v155","v156","v157","v158","v159","v160","v161","v162","v163","v164","v165","v166","v167","v168","v169","v170","v171","v172","v173","v174","v175","v176","v177","v178","v179","v180","v181","v182","v183","v184","v185","v186","v187","v188","v189","v190","v191","v192","v193","v194","v195","v196","v197","v198","v199","v200","v201","v202","v203","v204","v205","v206","v207","v208","v209","v210","v211","v212","v213","v214","v215","v216","v217","v218","v219","v220","v221","v222","v223","v224","v225","v226","v227","v228","v229","v230","v231","v232","v233","v234","v235","v236","v237","v238","v239","v240","v241","v242","v243","v244","v245","v246","v247","v248","v249","v250","v251","v252","v253","v254","v255","s40","s41","s42","s43","s44","s45","s46","s47","s48","s49","s50","s51","s52","s53","s54","s55","s56","s57","s58","s59","s60","s61","s62","s63","s64","s65","s66","s67","s68","s69","s70","s71","s72","s73","s74","s75","s76","s77","s78","s79","s80","s81","s82","s83"
#define K1X_CLOB "v88","v89","v104","v105","v106","v107","v108","v109","v110","v111","v112","v113","v114","v115","v116","v117","v118","v119","v120","v121","v122","v123","v124","v125","v126","v127","v128","v129","v130","v131","v132","v133","v134","v135","v136","v137","v138","v139","v140","v141","v142","v143","v144","v145","v146","v147","v148","v149","v150","v151","v152","v153","v154","v155","v156","v157","v158","v159","v160","v161","v162","v163","v164","v165","v166","v167","v168","v169","v170","v171","v172","v173","v174","v175","v176","v177","v178","v179","v180","v181","v182","v183","v184","v185","v186","v187","v188","v189","v190","v191","v192","v193","v194","v195","v196","v197","v198","v199","v200","v201","v202","v203","v204","v205","v206","v207","v208","v209","v210","v211","v212","v213","v214","v215","v216","v217","v218","v219","v220","v221","v222","v223","v224","v225","v226","v227","v228","v229","v230","v231","v232","v233","v234","v235","v236","v237","v238","v239","v240","v241","v242","v243","v244","v245","v246","v247","v248","v249","v250","v251","v252","v253","v254","v255"
#define K1A_CLOB "v101","v216","v218","v219","v220","v221","v222","v223","s40","s41","s42","s43","s44","s45","s46","s47","s54"
#define MFMA16(a,b,c) __builtin_amdgcn_mfma_f32_16x16x32_bf16(__builtin_bit_cast(__attribute__((ext_vector_type(8))) __bf16,(a)),__builtin_bit_cast(__attribute__((ext_vector_type(8))) __bf16,(b)),(c),0,0,0)
#define SB() __builtin_amdgcn_sched_barrier(0)
#define BAR() do{ SB(); asm volatile("s_waitcnt lgkmcnt(0)\n\ts_barrier":::"memory"); SB(); }while(0)
struct cp2{ f32x2 r,i; };
DI cp2 cmul(cp2 a,cp2 b){ cp2 o; o.r=a.r*b.r-a.i*b.i; o.i=a.r*b.i+a.i*b.r; return o; }
DI cp2 cexp2(f32x2 zr2,f32x2 zrev,float m){
  cp2 o;
#pragma unroll
  for(int k=0;k<2;k++){ const float mag=__builtin_amdgcn_exp2f(zr2[k]*m), fr=__builtin_amdgcn_fractf(zrev[k]*m); o.r[k]=mag*__builtin_amdgcn_cosf(fr); o.i[k]=mag*__builtin_amdgcn_sinf(fr); }
  return o; }
template<int SH> DI void expand16(const unsigned (&v)[9],unsigned (&o)[8]){
#pragma unroll
  for(int j=0;j<8;j++){
    const int t0=15-2*j, t1=14-2*j;
    const int e0=SH?((t0+(1<<(SH-1))-1+((t0>>SH)&1))>>SH):t0, e1=SH?((t1+(1<<(SH-1))-1+((t1>>SH)&1))>>SH):t1;
    const unsigned sel=((e0&1)?0x0302u:0x0100u)|(((e1&1)?0x0706u:0x0504u)<<16);
    o[j]=__builtin_amdgcn_perm(v[e1>>1],v[e0>>1],sel);
  }
}
constexpr int TROW=528, O_BIAS=73728, O_F=77824, FCH=1312, O_X=90112, KRS=4224;

extern "C" __global__ __launch_bounds__(512,2) void k1_mlp(const u16* __restrict__ x,const u16* __restrict__ w1,const u16* __restrict__ b1,const u16* __restrict__ w2,const u16* __restrict__ gw,
                                                const u16* __restrict__ logA,const u16* __restrict__ Aim,const u16* __restrict__ Cre,const u16* __restrict__ Cim,const u16* __restrict__ logdt,const u16* __restrict__ Dsk,
                                                u16* __restrict__ P,float* __restrict__ G,u16* __restrict__ XT,char* __restrict__ KR
){
  __shared__ __attribute__((aligned(1024))) char smem[163840];
#define TS(i)
  const unsigned lds=(unsigned)(size_t)smem;
  const unsigned b=blockIdx.x, tid=threadIdx.x, lane=tid&63, w=rfl(tid>>6);
  const unsigned xc=b&7, j=b>>3; const unsigned s=(xc&3)*2+(j&1), rt=(xc>>2)*16+(j>>1);
  const unsigned r=lane&15, g=lane>>4, q=r>>2, p=r&3, rh=w&1, mq=w>>1;
  const unsigned pg=(rt&15)>>1, pm=((rt&1)+2*(rt>>4)+(rt>>1))&3;
  const bool has=w<4||((w==5||w==6)&&pm==0);
  const unsigned kc=w<4?mq:2, kh=w<4?rh:(w==6), dch=w<4?256*mq+b:512+8*s+pg;
  const unsigned lr=lane>>3, rho=8*w+lr, vo=((rho&32)+2*(rho&15)+((rho>>4)&1))*1152+16*((lane&7)^((rho>>1)&7));
  const unsigned vw0=lane<10?min(36*(rt&15)+5*w+(lane>>1),575u)*9216+1152*s+128*(lane&1):0x7ffffff0u;
  asm volatile(
    "s_getpc_b64 s[40:41]\n\t"
    "s_add_u32 s40, s40, k1_mlp@rel32@lo+4\n\t"
    "s_addc_u32 s41, s41, k1_mlp@rel32@hi+12\n\t"
    "s_movk_i32 s42, 0x3c00\n\t"
    "s_mov_b32 s43, 0x20000\n\t"
    "buffer_load_dword v101, %[vcp], s[40:43], 0 offen\n\t"
    "global_load_dword v218, %[vp], %[pla]\n\t"
    "global_load_ushort v222, %[vd], %[pdt]\n\t"
    "global_load_dword v219, %[vp], %[pai]\n\t"
    "global_load_dword v220, %[vp], %[pcr]\n\t"
    "global_load_dword v221, %[vp], %[pci]\n\t"
    "global_load_ushort v223, %[vd], %[pdk]\n\t"
    "s_mov_b64 s[40:41], %[pb1]\n\t"
    "s_movk_i32 s42, 0x2400\n\t"
    "s_mov_b32 s43, 0x20000\n\t"
    "s_mov_b64 s[44:45], %[px]\n\t"
    "s_mov_b32 s46, 0x240000\n\t"
    "s_mov_b32 s47, 0x20000\n\t"
    "buffer_load_dword v216, %[o1], s[40:43], 0 offen\n\t"
    "s_add_u32 m0, %[ldsx], 0x0\n\t"
    "s_add_u32 s54, %[sxt], 0x0\n\t"
    "buffer_load_dwordx4 %[vo], s[44:47], s54 offen lds\n\t"
    "s_add_u32 m0, %[ldsx], 0x2000\n\t"
    "s_add_u32 s54, %[sxt], 0x80\n\t"
    "buffer_load_dwordx4 %[vo], s[44:47], s54 offen lds\n\t"
    "s_add_u32 m0, %[ldsx], 0x4000\n\t"
    "s_add_u32 s54, %[sxt], 0x100\n\t"
    "buffer_load_dwordx4 %[vo], s[44:47], s54 offen lds\n\t"
    "s_add_u32 m0, %[ldsx], 0x6000\n\t"
    "s_add_u32 s54, %[sxt], 0x180\n\t"
    "buffer_load_dwordx4 %[vo], s[44:47], s54 offen lds\n\t"
    "s_add_u32 m0, %[ldsx], 0x8000\n\t"
    "s_add_u32 s54, %[sxt], 0x200\n\t"
    "buffer_load_dwordx4 %[vo], s[44:47], s54 offen lds\n\t"
    "s_add_u32 m0, %[ldsx], 0xa000\n\t"
    "s_add_u32 s54, %[sxt], 0x280\n\t"
    "buffer_load_dwordx4 %[vo], s[44:47], s54 offen lds\n\t"
    "s_add_u32 m0, %[ldsx], 0xc000\n\t"
    "s_add_u32 s54, %[sxt], 0x300\n\t"
    "buffer_load_dwordx4 %[vo], s[44:47], s54 offen lds\n\t"
    "s_add_u32 m0, %[ldsx], 0xe000\n\t"
    "s_add_u32 s54, %[sxt], 0x380\n\t"
    "buffer_load_dwordx4 %[vo], s[44:47], s54 offen lds\n\t"
    "s_add_u32 m0, %[ldsx], 0x10000\n\t"
    "s_add_u32 s54, %[sxt], 0x400\n\t"
    "buffer_load_dwordx4 %[vo], s[44:47], s54 offen lds\n\t"
    "s_mov_b64 s[40:41], %[pw1]\n\t"
    "s_mov_b32 s42, 0x510000\n\t"
    "buffer_load_dword v101, %[vw0], s[40:43], 0 offen\n\t"
    :: [pb1]"s"(b1),[px]"s"(x),[pw1]"s"(w1),[pla]"s"(logA),[pai]"s"(Aim),[pcr]"s"(Cre),[pci]"s"(Cim),[pdt]"s"(logdt),[pdk]"s"(Dsk),[vp]"v"(dch*256+lane*4),[vd]"v"(dch*2),[o1]"v"(s*1152+tid*4),[ldsx]"s"(lds+O_X+w*1024),[sxt]"s"(rt*73728),[vo]"v"(vo),[vw0]"v"(vw0),[vcp]"v"(tid*128)
    : "memory","scc",K1A_CLOB);
  const unsigned fw=((lr>>1)&1)|((w&1)<<1), cs=16*((lane&7)^(fw<<1));
  const unsigned vw1=(8*w+lr)*9216+cs+1152*s;
  char* T=smem+TROW*56*kc;
  u16* Fc=(u16*)(smem+O_F+kc*FCH);
  const unsigned ft=((q>>1)&1)|((g&1)<<1), sw=(r>>1)&7;
  const unsigned xrow=lds+O_X+128*(32*rh+r);
  const unsigned vxb0=xrow+16*(g^sw), vxb1=xrow+16*(g^sw^4);
  const unsigned xtt=w<4?s+8*w:32+(s&3), vxt0=lds+O_X+8192*(xtt>>2)+128*(16*(q&1)+4*g+(q>>1))+8*(p&1);
  const unsigned pl=min(lane,47u), prw=192*(pm?pm-1:0)+24*w+pl%24, pbk=2*pg+2+pl/24;
  const char* apf=pbk<9?(const char*)w1+prw*9216+1152*s+128*pbk:(const char*)w2+(576*s+prw)*1152+128*(pbk-9);
  const unsigned when=pm==0?100:(pg<=3?0:pg-3+1);
  const unsigned grow=64*rt+8*s+w, vw2=(576*s+8*w+lr)*1152+cs;
  const unsigned vtr=lds+(8*g+q)*128+16*((2*mq+(p>>1))^(ft<<1))+8*(p&1);
  const unsigned vact=xrow+16*((2*mq+(g>>1))^sw)+8*(g&1)+65536;
  const unsigned vbias=lds+O_BIAS+(16*mq+4*g)*4;
  const unsigned vst=((s*144+4*mq+g)*2048+64*rt+32*rh+2*r)*8;
  const unsigned vxs=(w<4||(w==4&&s<4))?(16*xtt+r)*4096+(64*rt+8*g)*2:0x7ffffff0u;
  asm volatile("v_mov_b32 v90, %0\n\tv_mov_b32 v91, %1\n\tv_mov_b32 v92, %2\n\tv_mov_b32 v93, %3\n\tv_mov_b32 v94, %4\n\tv_mov_b32 v95, %5\n\tv_mov_b32 v96, %6\n\t"
               "v_mov_b32 v97, %7\n\tv_mov_b32 v98, %8\n\tv_mov_b32 v99, %9\n\tv_mov_b32 v100, %10\n\tv_mov_b64 v[102:103], %11"
    ::"v"(vxt0+16*((2*(xtt&3)+(p>>1))^(2*g))),"v"(vxt0+256+16*((2*(xtt&3)+(p>>1))^(2*g+1))),"v"(vxb0),"v"(vxb1),"v"(vtr),"v"(vact),"v"(vbias),"v"(vw1),"v"(vw2),"v"(vst),"v"(vxs),"v"(apf)
    :"v90","v91","v92","v93","v94","v95","v96","v97","v98","v99","v100","v102","v103");
  unsigned pa,pb,pc,pd,pt,pk;
  asm volatile("s_waitcnt vmcnt(11)\n\tv_mov_b32 %0, v218\n\tv_mov_b32 %1, v219\n\tv_mov_b32 %2, v220\n\tv_mov_b32 %3, v221\n\tv_mov_b32 %4, v222\n\tv_mov_b32 %5, v223"
    :"=v"(pa),"=v"(pb),"=v"(pc),"=v"(pd),"=v"(pt),"=v"(pk)::"memory");
  TS(0);
  if(has){
    const float dt=rbf(__builtin_amdgcn_exp2f(blo(pt)*1.44269504089f));
    const f32x2 a={rbf(__builtin_amdgcn_exp2f(blo(pa)*1.44269504089f)),rbf(__builtin_amdgcn_exp2f(bhi(pa)*1.44269504089f))};
    const f32x2 zr2=(-a*dt)*1.44269504089f, zrev=(f32x2{blo(pb),bhi(pb)}*dt)*0.15915494309f;
    const cp2 h0{(f32x2{blo(pc),bhi(pc)}*dt)*2.f,(f32x2{blo(pd),bhi(pd)}*dt)*-2.f}, w128=cexp2(zr2,-zrev,128.f);
    auto put=[&](int row,cp2 v){ *(u32x2*)(T+8*lane+row*TROW)=u32x2{cvtpk(v.r[0],v.i[0]),cvtpk(v.r[1],v.i[1])}; };
    if(kh==0){
      const cp2 w8=cexp2(zr2,zrev,8.f), w1c=cexp2(zr2,-zrev,1.f);
      cp2 e=w8, v0=h0, v1=cmul(h0,w128);
      put(1,e); put(16,v0); put(24,v1);
#pragma unroll
      for(int i=1;i<8;i++){ e=cmul(e,w8); put(2*i,e); v0=cmul(v0,w1c); put(16+i,v0); e=cmul(e,w8); put(2*i+1,e); v1=cmul(v1,w1c); put(24+i,v1); }
    } else {
      const cp2 w2c=cexp2(zr2,-zrev,2.f), w4c=cexp2(zr2,-zrev,4.f);
      cp2 h=cmul(cmul(h0,w128),w128);
#pragma unroll
      for(int i=2;i<17;i++){
        if(i<4){ cp2 v=h; put(32+4*(i-2),v);
#pragma unroll
          for(int k=1;k<4;k++){ v=cmul(v,w2c); put(32+4*(i-2)+k,v); } }
        else if(i<8){ put(40+2*(i-4),h); put(41+2*(i-4),cmul(h,w4c)); }
        else put(48+(i-8),h);
        if(i<16) h=cmul(h,w128);
      }
    }
    } else if(w==7) *(u32x2*)(smem+8*lane)=u32x2{0x3f80u,0x3f80u};
  BAR();
  TS(1);
  unsigned f0=0,f1=0,f2=0,f3=0;
  if(has){
    const char* Tr=T+r*TROW+16*g; const char* Tb=(r?Tr:smem+16*g);
    f32x4 a1={(lane==0&&kh==0)?blo(pk):0.f,0,0,0}, a2={0,0,0,0};
    if(kh==0){
      u32x4 aq[8],bq[8];
#pragma unroll
      for(int ks=0;ks<8;ks++){ bq[ks]=*(const u32x4*)(Tb+64*ks); aq[ks]=*(const u32x4*)(Tr+16*TROW+64*ks); }
      SB();
#pragma unroll
      for(int ks=0;ks<8;ks++){ if(ks&1) a2=MFMA16(aq[ks],bq[ks],a2); else a1=MFMA16(aq[ks],bq[ks],a1); }
      a1+=a2;
    } else {
      u32x4 aq[5],bq[5],cq[5];
#pragma unroll
      for(int ks=0;ks<5;ks++){ bq[ks]=*(const u32x4*)(Tb+64*ks); aq[ks]=*(const u32x4*)(Tr+32*TROW+64*ks); cq[ks]=*(const u32x4*)(Tr+48*TROW+64*ks); }
      SB();
#pragma unroll
      for(int ks=0;ks<8;ks++){ const int z=ks%5; a1=MFMA16(aq[z],bq[z],a1); a2=MFMA16(cq[z],bq[z],a2); SB();
        if(ks<3){ bq[z]=*(const u32x4*)(Tb+64*(ks+5)); aq[z]=*(const u32x4*)(Tr+32*TROW+64*(ks+5)); cq[z]=*(const u32x4*)(Tr+48*TROW+64*(ks+5)); SB(); } }
    }
    f0=cvtpk(a1[0],a1[1]); f1=cvtpk(a1[2],a1[3]); f2=cvtpk(a2[0],a2[1]); f3=cvtpk(a2[2],a2[3]);
  }
  asm volatile("":"+v"(f0),"+v"(f1),"+v"(f2),"+v"(f3));
  BAR();
  TS(2);
  asm volatile(
    "s_mov_b64 s[40:41], %[pw1]\n\t"
    "s_mov_b32 s42, 0x510000\n\t"
    "s_mov_b32 s43, 0x20000\n\t"
    "s_add_u32 m0, %[ldsw], 0x0\n\t"
    "s_mov_b32 s54, 0x0\n\t"
    "buffer_load_dwordx4 v97, s[40:43], s54 offen lds\n\t"
    "s_add_u32 m0, %[ldsw], 0x2000\n\t"
    "s_mov_b32 s54, 0x90000\n\t"
    "buffer_load_dwordx4 v97, s[40:43], s54 offen lds\n\t"
    "s_add_u32 m0, %[ldsw], 0x4000\n\t"
    "s_mov_b32 s54, 0x120000\n\t"
    "buffer_load_dwordx4 v97, s[40:43], s54 offen lds\n\t"
    "s_add_u32 m0, %[ldsw], 0x6000\n\t"
    "s_mov_b32 s54, 0x1b0000\n\t"
    "buffer_load_dwordx4 v97, s[40:43], s54 offen lds\n\t"
    "s_add_u32 m0, %[ldsw], 0x8000\n\t"
    "s_mov_b32 s54, 0x240000\n\t"
    "buffer_load_dwordx4 v97, s[40:43], s54 offen lds\n\t"
    "s_add_u32 m0, %[ldsw], 0xa000\n\t"
    "s_mov_b32 s54, 0x2d0000\n\t"
    "buffer_load_dwordx4 v97, s[40:43], s54 offen lds\n\t"
    "s_add_u32 m0, %[ldsw], 0xc000\n\t"
    "s_mov_b32 s54, 0x360000\n\t"
    "buffer_load_dwordx4 v97, s[40:43], s54 offen lds\n\t"
    "s_add_u32 m0, %[ldsw], 0xe000\n\t"
    "s_mov_b32 s54, 0x3f0000\n\t"
    "buffer_load_dwordx4 v97, s[40:43], s54 offen lds\n\t"
    "s_waitcnt vmcnt(18)\n\t"
    "v_lshlrev_b32_e32 v218, 16, v216\n\t"
    "v_and_b32_e32 v219, 0xffff0000, v216\n\t"
    "v_mul_f32_e32 v218, 0x3fb8aa3b, v218\n\t"
    "v_mul_f32_e32 v219, 0x3fb8aa3b, v219\n\t"
    "ds_write_b64 %[vbw], v[218:219]\n\t"
    :: [pw1]"s"(w1),[ldsw]"s"(lds+w*1024),[vbw]"v"(lds+O_BIAS+tid*8)
    : "memory","scc",K1A_CLOB);
  if(has){
    if(kh==0) *(u32x2*)(Fc+128*(g>>1)+8*r+4*(g&1))=u32x2{f0,f1};
    else if(g<2){ *(u32x2*)(Fc+256+64*g+4*r)=u32x2{f0,f1};
                  u16* f=Fc+512+64*g+r; f[0]=(u16)f2; f[16]=(u16)(f2>>16); f[32]=(u16)f3; f[48]=(u16)(f3>>16); }
    else { unsigned* f=(unsigned*)(Fc+384+64*(g-2)+2*r); f[0]=f0; f[16]=f1; if(lane==32) Fc[640]=(u16)f2; }
  }
  SB();
  TS(3);
  asm volatile(
    "s_waitcnt vmcnt(9) lgkmcnt(0)\n\ts_barrier\n\t"
    "v_add_u32_e32 v88, 0x8000, v92\n\t"
    "v_add_u32_e32 v89, 0x8000, v93\n\t"
    "ds_read_b64_tr_b16 v[104:105], v90 offset:0\n\t"
    "ds_read_b64_tr_b16 v[106:107], v91 offset:0\n\t"
    "ds_read_b64_tr_b16 v[108:109], v90 offset:4096\n\t"
    "ds_read_b64_tr_b16 v[110:111], v91 offset:4096\n\t"
    "ds_read_b128 v[112:115], v92 offset:0\n\t"
    "ds_read_b128 v[116:119], v92 offset:2048\n\t"
    "ds_read_b128 v[120:123], v93 offset:0\n\t"
    "ds_read_b128 v[124:127], v93 offset:2048\n\t"
    "ds_read_b128 v[128:131], v92 offset:8192\n\t"
    "ds_read_b128 v[132:135], v92 offset:10240\n\t"
    "ds_read_b128 v[136:139], v93 offset:8192\n\t"
    "ds_read_b128 v[140:143], v93 offset:10240\n\t"
    "ds_read_b128 v[144:147], v92 offset:16384\n\t"
    "ds_read_b128 v[148:151], v92 offset:18432\n\t"
    "ds_read_b128 v[152:155], v93 offset:16384\n\t"
    "ds_read_b128 v[156:159], v93 offset:18432\n\t"
    "ds_read_b128 v[160:163], v92 offset:24576\n\t"
    "ds_read_b128 v[164:167], v92 offset:26624\n\t"
    "ds_read_b128 v[168:171], v93 offset:24576\n\t"
    "ds_read_b128 v[172:175], v93 offset:26624\n\t"
    "ds_read_b128 v[176:179], v88 offset:0\n\t"
    "ds_read_b128 v[180:183], v88 offset:2048\n\t"
    "ds_read_b128 v[184:187], v89 offset:0\n\t"
    "ds_read_b128 v[188:191], v89 offset:2048\n\t"
    "ds_read_b128 v[192:195], v88 offset:8192\n\t"
    "ds_read_b128 v[196:199], v88 offset:10240\n\t"
    "ds_read_b128 v[200:203], v89 offset:8192\n\t"
    "ds_read_b128 v[204:207], v89 offset:10240\n\t"
    "ds_read_b128 v[208:211], v88 offset:16384\n\t"
    "ds_read_b128 v[212:215], v88 offset:18432\n\t"
    "ds_read_b128 v[216:219], v89 offset:16384\n\t"
    "ds_read_b128 v[220:223], v89 offset:18432\n\t"
    "ds_read_b128 v[224:227], v88 offset:24576\n\t"
    "ds_read_b128 v[228:231], v88 offset:26624\n\t"
    "ds_read_b128 v[232:235], v89 offset:24576\n\t"
    "ds_read_b128 v[236:239], v89 offset:26624\n\t"
    "ds_read_b128 v[240:243], v88 offset:32768\n\t"
    "ds_read_b128 v[244:247], v88 offset:34816\n\t"
    "ds_read_b128 v[248:251], v89 offset:32768\n\t"
    "ds_read_b128 v[252:255], v89 offset:34816\n\t"
    ::
    : "memory","scc","s40","s41","s42","s43","s54",K1X_CLOB);
  TS(4);
  asm volatile(
    "v_mov_b32_e32 v18, v92\n\t"
    "v_mov_b32_e32 v19, v93\n\t"
    "v_mov_b32_e32 v20, v94\n\t"
    "v_mov_b32_e32 v21, v97\n\t"
    "v_mov_b32_e32 v22, v98\n\t"
    "v_mov_b32_e32 v23, v99\n\t"
    "s_mov_b64 s[40:41], %[pw1]\n\t"
    "s_mov_b32 s42, 0x510000\n\t"
    "s_mov_b32 s43, 0x20000\n\t"
    "s_mov_b64 s[44:45], %[pw2]\n\t"
    "s_mov_b32 s46, 0x510000\n\t"
    "s_mov_b32 s47, 0x20000\n\t"
    "s_mov_b64 s[60:61], %[pw2]\n\t"
    "s_mov_b32 s62, 0x510000\n\t"
    "s_mov_b32 s63, 0x20000\n\t"
    "s_mov_b64 s[48:49], %[pp]\n\t"
    "s_mov_b32 s50, 0x1200000\n\t"
    "s_mov_b32 s51, 0x20000\n\t"
    "s_mov_b32 s52, 0\n\t"
    "s_mov_b32 s53, 0\n\t"
    "s_mov_b32 s56, 0x3fb8aa3b\n\t"
    "s_mov_b32 s57, %[ldsw]\n\t"
    "s_mov_b32 s58, 0xffff0000\n\t"
    "s_mov_b64 s[72:73], %[pxt]\n\t"
    "s_mov_b32 s74, 0x240000\n\t"
    "s_mov_b32 s75, 0x20000\n\t"
    "s_waitcnt lgkmcnt(15)\n\t"
    "buffer_store_dwordx4 v[104:107], v100, s[72:75], 0 offen\n\t"
    "buffer_store_dwordx4 v[108:111], v100, s[72:75], 0 offen offset:64\n\t"
    "s_mov_b32 s83, %[when]\n\t"
    "s_nop 0\n\t"
    "s_cmp_eq_u32 s83, 0\n\t"
    "s_cbranch_scc0 Lr0_%=\n\t"
    "global_load_dword v101, v[102:103], off\n\t"
    "Lr0_%=:\n\t"
    "v_mbcnt_lo_u32_b32 v28, -1, 0\n\t"
    "v_mbcnt_hi_u32_b32 v28, -1, v28\n\t"
    "v_add_u32_e32 v104, 0x8000, v20\n\t"
    "v_add_u32_e32 v105, 0x8000, v18\n\t"
    "v_add_u32_e32 v106, 0x8000, v19\n\t"
    "v_lshlrev_b32_e32 v107, 4, v28\n\t"
    "v_cmp_eq_u32_e32 vcc, 63, v28\n\t"
    "v_mov_b32_e32 v108, %[sg4]\n\t"
    "v_mov_b32_e32 v28, 0x7ffffff0\n\t"
    "v_cndmask_b32_e32 v108, v28, v108, vcc\n\t"
    "v_mov_b32_e32 v26, v95\n\t"
    "v_mov_b32_e32 v27, v96\n\t"
    "s_waitcnt vmcnt(8) lgkmcnt(0)\n\t"
    "s_barrier\n\t"
    "ds_read_b64_tr_b16 v[76:77], v20 offset:0\n\t"
    "ds_read_b64_tr_b16 v[78:79], v20 offset:512\n\t"
    "ds_read_b64_tr_b16 v[80:81], v20 offset:4096\n\t"
    "ds_read_b64_tr_b16 v[82:83], v20 offset:4608\n\t"
    "ds_read_b64_tr_b16 v[84:85], v20 offset:8192\n\t"
    "ds_read_b64_tr_b16 v[86:87], v20 offset:8704\n\t"
    "ds_read_b64_tr_b16 v[88:89], v20 offset:12288\n\t"
    "ds_read_b64_tr_b16 v[90:91], v20 offset:12800\n\t"
    "s_mov_b32 s55, 4\n\t"
    "LA_%=:\n\t"
    "s_waitcnt lgkmcnt(6)\n\t"
    "v_mfma_f32_16x16x32_bf16 v[60:63], v[76:79], v[112:115], 0\n\t"
    "s_waitcnt vmcnt(4) lgkmcnt(4)\n\t"
    "s_barrier\n\t"
    "ds_read_b64_tr_b16 v[92:93], v20 offset:16384\n\t"
    "v_mfma_f32_16x16x32_bf16 v[64:67], v[76:79], v[116:119], 0\n\t"
    "ds_read_b64_tr_b16 v[94:95], v20 offset:16896\n\t"
    "s_sub_u32 s83, s83, 1\n\t"
    "s_cmp_eq_u32 s83, 0\n\t"
    "s_cbranch_scc0 LrA_%=\n\t"
    "global_load_dword v101, v[102:103], off \n\t"
    "LrA_%=:\n\t"
    "s_add_u32 m0, s57, 0x10000\n\t"
    "s_add_u32 s54, s53, 0x480000\n\t"
    "buffer_load_dwordx4 v21, s[40:43], s54 offen  lds\n\t"
    "v_mfma_f32_16x16x32_bf16 v[60:63], v[80:83], v[120:123], v[60:63]\n\t"
    "ds_read_b64_tr_b16 v[96:97], v20 offset:20480\n\t"
    "v_mfma_f32_16x16x32_bf16 v[64:67], v[80:83], v[124:127], v[64:67]\n\t"
    "ds_read_b64_tr_b16 v[98:99], v20 offset:20992\n\t"
    "v_fma_f32 v28, v68, s56, v56\n\t"
    "v_fma_f32 v29, v69, s56, v57\n\t"
    "s_waitcnt lgkmcnt(6)\n\t"
    "v_mfma_f32_16x16x32_bf16 v[60:63], v[84:87], v[128:131], v[60:63]\n\t"
    "ds_read_b64_tr_b16 v[76:77], v20 offset:24576\n\t"
    "v_fma_f32 v30, v70, s56, v58\n\t"
    "v_fma_f32 v31, v71, s56, v59\n\t"
    "v_mfma_f32_16x16x32_bf16 v[64:67], v[84:87], v[132:135], v[64:67]\n\t"
    "ds_read_b64_tr_b16 v[78:79], v20 offset:25088\n\t"
    "v_exp_f32_e32 v36, v28\n\t"
    "v_exp_f32_e32 v37, v29\n\t"
    "s_add_u32 m0, s57, 0x0\n\t"
    "s_add_u32 s54, s53, 0x80\n\t"
    "buffer_load_dwordx4 v21, s[40:43], s54 offen  lds\n\t"
    "s_waitcnt lgkmcnt(6)\n\t"
    "v_mfma_f32_16x16x32_bf16 v[60:63], v[88:91], v[136:139], v[60:63]\n\t"
    "ds_read_b64_tr_b16 v[80:81], v20 offset:28672\n\t"
    "v_exp_f32_e32 v38, v30\n\t"
    "v_mfma_f32_16x16x32_bf16 v[64:67], v[88:91], v[140:143], v[64:67]\n\t"
    "ds_read_b64_tr_b16 v[82:83], v20 offset:29184\n\t"
    "v_exp_f32_e32 v39, v31\n\t"
    "s_waitcnt lgkmcnt(6)\n\t"
    "v_mfma_f32_16x16x32_bf16 v[60:63], v[92:95], v[144:147], v[60:63]\n\t"
    "s_waitcnt vmcnt(4) lgkmcnt(4)\n\t"
    "s_barrier\n\t"
    "ds_read_b64_tr_b16 v[84:85], v104 offset:0\n\t"
    "v_fma_f32 v44, v36, 0.5, 1.0\n\t"
    "v_fma_f32 v45, v37, 0.5, 1.0\n\t"
    "v_mfma_f32_16x16x32_bf16 v[64:67], v[92:95], v[148:151], v[64:67]\n\t"
    "ds_read_b64_tr_b16 v[86:87], v104 offset:512\n\t"
    "v_fma_f32 v46, v38, 0.5, 1.0\n\t"
    "v_fma_f32 v47, v39, 0.5, 1.0\n\t"
    "s_add_u32 m0, s57, 0x2000\n\t"
    "s_add_u32 s54, s53, 0x90080\n\t"
    "buffer_load_dwordx4 v21, s[40:43], s54 offen  lds\n\t"
    "v_mfma_f32_16x16x32_bf16 v[60:63], v[96:99], v[152:155], v[60:63]\n\t"
    "ds_read_b64_tr_b16 v[88:89], v104 offset:4096\n\t"
    "v_fma_f32 v44, v36, v44, 1.0\n\t"
    "v_fma_f32 v45, v37, v45, 1.0\n\t"
    "v_fma_f32 v46, v38, v46, 1.0\n\t"
    "v_mfma_f32_16x16x32_bf16 v[64:67], v[96:99], v[156:159], v[64:67]\n\t"
    "ds_read_b64_tr_b16 v[90:91], v104 offset:4608\n\t"
    "v_fma_f32 v47, v39, v47, 1.0\n\t"
    "v_rcp_f32_e32 v44, v44\n\t"
    "s_waitcnt lgkmcnt(6)\n\t"
    "v_mfma_f32_16x16x32_bf16 v[60:63], v[76:79], v[160:163], v[60:63]\n\t"
    "ds_read_b64_tr_b16 v[92:93], v104 offset:8192\n\t"
    "v_rcp_f32_e32 v45, v45\n\t"
    "v_mfma_f32_16x16x32_bf16 v[64:67], v[76:79], v[164:167], v[64:67]\n\t"
    "ds_read_b64_tr_b16 v[94:95], v104 offset:8704\n\t"
    "v_rcp_f32_e32 v46, v46\n\t"
    "s_add_u32 m0, s57, 0x4000\n\t"
    "s_add_u32 s54, s53, 0x120080\n\t"
    "buffer_load_dwordx4 v21, s[40:43], s54 offen  lds\n\t"
    "s_waitcnt lgkmcnt(6)\n\t"
    "v_mfma_f32_16x16x32_bf16 v[60:63], v[80:83], v[168:171], v[60:63]\n\t"
    "ds_read_b64_tr_b16 v[96:97], v104 offset:12288\n\t"
    "v_rcp_f32_e32 v47, v47\n\t"
    "v_mfma_f32_16x16x32_bf16 v[64:67], v[80:83], v[172:175], v[64:67]\n\t"
    "ds_read_b64_tr_b16 v[98:99], v104 offset:12800\n\t"
    "v_fma_f32 v44, -v28, v44, v28\n\t"
    "v_fma_f32 v45, -v29, v45, v29\n\t"
    "v_fma_f32 v46, -v30, v46, v30\n\t"
    "s_waitcnt lgkmcnt(6)\n\t"
    "v_mfma_f32_16x16x32_bf16 v[60:63], v[84:87], v[176:179], v[60:63]\n\t"
    "s_waitcnt vmcnt(4) lgkmcnt(4)\n\t"
    "s_barrier\n\t"
    "ds_read_b64_tr_b16 v[76:77], v104 offset:16384\n\t"
    "v_fma_f32 v47, -v31, v47, v31\n\t"
    "v_cvt_pk_bf16_f32 v52, v44, v45\n\t"
    "v_mfma_f32_16x16x32_bf16 v[64:67], v[84:87], v[180:183], v[64:67]\n\t"
    "ds_read_b64_tr_b16 v[78:79], v104 offset:16896\n\t"
    "v_cvt_pk_bf16_f32 v53, v46, v47\n\t"
    "ds_write_b64 v26, v[52:53] offset:0\n\t"
    "s_add_u32 m0, s57, 0x6000\n\t"
    "s_add_u32 s54, s53, 0x1b0080\n\t"
    "buffer_load_dwordx4 v21, s[40:43], s54 offen  lds\n\t"
    "v_mfma_f32_16x16x32_bf16 v[60:63], v[88:91], v[184:187], v[60:63]\n\t"
    "ds_read_b64_tr_b16 v[80:81], v104 offset:20480\n\t"
    "v_fma_f32 v32, v72, s56, v56\n\t"
    "v_fma_f32 v33, v73, s56, v57\n\t"
    "v_fma_f32 v34, v74, s56, v58\n\t"
    "v_mfma_f32_16x16x32_bf16 v[64:67], v[88:91], v[188:191], v[64:67]\n\t"
    "ds_read_b64_tr_b16 v[82:83], v104 offset:20992\n\t"
    "v_fma_f32 v35, v75, s56, v59\n\t"
    "v_exp_f32_e32 v40, v32\n\t"
    "s_waitcnt lgkmcnt(7)\n\t"
    "v_mfma_f32_16x16x32_bf16 v[60:63], v[92:95], v[192:195], v[60:63]\n\t"
    "ds_read_b64_tr_b16 v[84:85], v104 offset:24576\n\t"
    "v_exp_f32_e32 v41, v33\n\t"
    "v_mfma_f32_16x16x32_bf16 v[64:67], v[92:95], v[196:199], v[64:67]\n\t"
    "ds_read_b64_tr_b16 v[86:87], v104 offset:25088\n\t"
    "v_exp_f32_e32 v42, v34\n\t"
    "s_add_u32 m0, s57, 0x8000\n\t"
    "s_add_u32 s54, s53, 0x240080\n\t"
    "buffer_load_dwordx4 v21, s[40:43], s54 offen  lds\n\t"
    "s_waitcnt lgkmcnt(7)\n\t"
    "v_mfma_f32_16x16x32_bf16 v[60:63], v[96:99], v[200:203], v[60:63]\n\t"
    "ds_read_b64_tr_b16 v[88:89], v104 offset:28672\n\t"
    "v_exp_f32_e32 v43, v35\n\t"
    "v_mfma_f32_16x16x32_bf16 v[64:67], v[96:99], v[204:207], v[64:67]\n\t"
    "ds_read_b64_tr_b16 v[90:91], v104 offset:29184\n\t"
    "v_fma_f32 v48, v40, 0.5, 1.0\n\t"
    "v_fma_f32 v49, v41, 0.5, 1.0\n\t"
    "s_waitcnt lgkmcnt(7)\n\t"
    "v_mfma_f32_16x16x32_bf16 v[60:63], v[76:79], v[208:211], v[60:63]\n\t"
    "s_waitcnt vmcnt(4) lgkmcnt(4)\n\t"
    "s_barrier\n\t"
    "ds_read_b64_tr_b16 v[92:93], v104 offset:32768\n\t"
    "ds_read_b128 v[56:59], v27\n\t"
    "v_add_u32_e32 v27, 0x100, v27\n\t"
    "v_fma_f32 v50, v42, 0.5, 1.0\n\t"
    "v_mfma_f32_16x16x32_bf16 v[64:67], v[76:79], v[212:215], v[64:67]\n\t"
    "ds_read_b64_tr_b16 v[94:95], v104 offset:33280\n\t"
    "v_fma_f32 v51, v43, 0.5, 1.0\n\t"
    "v_fma_f32 v48, v40, v48, 1.0\n\t"
    "s_add_u32 m0, s57, 0xa000\n\t"
    "s_add_u32 s54, s53, 0x2d0080\n\t"
    "buffer_load_dwordx4 v21, s[40:43], s54 offen  lds\n\t"
    "v_mfma_f32_16x16x32_bf16 v[60:63], v[80:83], v[216:219], v[60:63]\n\t"
    "ds_read_b64_tr_b16 v[96:97], v104 offset:36864\n\t"
    "v_fma_f32 v49, v41, v49, 1.0\n\t"
    "v_fma_f32 v50, v42, v50, 1.0\n\t"
    "v_fma_f32 v51, v43, v51, 1.0\n\t"
    "v_mfma_f32_16x16x32_bf16 v[64:67], v[80:83], v[220:223], v[64:67]\n\t"
    "ds_read_b64_tr_b16 v[98:99], v104 offset:37376\n\t"
    "v_rcp_f32_e32 v48, v48\n\t"
    "s_waitcnt lgkmcnt(7)\n\t"
    "v_mfma_f32_16x16x32_bf16 v[60:63], v[84:87], v[224:227], v[60:63]\n\t"
    "ds_read_b64_tr_b16 v[76:77], v20 offset:0\n\t"
    "v_rcp_f32_e32 v49, v49\n\t"
    "v_mfma_f32_16x16x32_bf16 v[64:67], v[84:87], v[228:231], v[64:67]\n\t"
    "ds_read_b64_tr_b16 v[78:79], v20 offset:512\n\t"
    "v_rcp_f32_e32 v50, v50\n\t"
    "s_add_u32 m0, s57, 0xc000\n\t"
    "s_add_u32 s54, s53, 0x360080\n\t"
    "buffer_load_dwordx4 v21, s[40:43], s54 offen  lds\n\t"
    "s_waitcnt lgkmcnt(7)\n\t"
    "v_mfma_f32_16x16x32_bf16 v[60:63], v[88:91], v[232:235], v[60:63]\n\t"
    "ds_read_b64_tr_b16 v[80:81], v20 offset:4096\n\t"
    "v_rcp_f32_e32 v51, v51\n\t"
    "v_fma_f32 v48, -v32, v48, v32\n\t"
    "v_mfma_f32_16x16x32_bf16 v[64:67], v[88:91], v[236:239], v[64:67]\n\t"
    "ds_read_b64_tr_b16 v[82:83], v20 offset:4608\n\t"
    "v_fma_f32 v49, -v33, v49, v33\n\t"
    "v_fma_f32 v50, -v34, v50, v34\n\t"
    "s_waitcnt lgkmcnt(6)\n\t"
    "v_mfma_f32_16x16x32_bf16 v[60:63], v[92:95], v[240:243], v[60:63]\n\t"
    "s_waitcnt vmcnt(4) lgkmcnt(4)\n\t"
    "s_barrier\n\t"
    "ds_read_b64_tr_b16 v[84:85], v20 offset:8192\n\t"
    "v_fma_f32 v51, -v35, v51, v35\n\t"
    "v_cvt_pk_bf16_f32 v54, v48, v49\n\t"
    "v_cvt_pk_bf16_f32 v55, v50, v51\n\t"
    "v_mfma_f32_16x16x32_bf16 v[64:67], v[92:95], v[244:247], v[64:67]\n\t"
    "ds_read_b64_tr_b16 v[86:87], v20 offset:8704\n\t"
    "ds_write_b64 v26, v[54:55] offset:2048\n\t"
    "v_add_u32_e32 v26, s58, v26\n\t"
    "s_movk_i32 s58, 0x2000\n\t"
    "s_add_u32 m0, s57, 0xe000\n\t"
    "s_add_u32 s54, s53, 0x3f0080\n\t"
    "buffer_load_dwordx4 v21, s[40:43], s54 offen  lds\n\t"
    "v_mfma_f32_16x16x32_bf16 v[68:71], v[96:99], v[248:251], v[60:63]\n\t"
    "ds_read_b64_tr_b16 v[88:89], v20 offset:12288\n\t"
    "v_mfma_f32_16x16x32_bf16 v[72:75], v[96:99], v[252:255], v[64:67]\n\t"
    "ds_read_b64_tr_b16 v[90:91], v20 offset:12800\n\t"
    "s_waitcnt lgkmcnt(7)\n\t"
    "v_mfma_f32_16x16x32_bf16 v[60:63], v[76:79], v[112:115], 0\n\t"
    "ds_read_b64_tr_b16 v[92:93], v20 offset:16384\n\t"
    "v_mfma_f32_16x16x32_bf16 v[64:67], v[76:79], v[116:119], 0\n\t"
    "ds_read_b64_tr_b16 v[94:95], v20 offset:16896\n\t"
    "s_add_u32 m0, s57, 0x10000\n\t"
    "s_add_u32 s54, s53, 0x480080\n\t"
    "buffer_load_dwordx4 v21, s[40:43], s54 offen  lds\n\t"
    "s_waitcnt lgkmcnt(7)\n\t"
    "v_mfma_f32_16x16x32_bf16 v[60:63], v[80:83], v[120:123], v[60:63]\n\t"
    "ds_read_b64_tr_b16 v[96:97], v20 offset:20480\n\t"
    "v_mfma_f32_16x16x32_bf16 v[64:67], v[80:83], v[124:127], v[64:67]\n\t"
    "ds_read_b64_tr_b16 v[98:99], v20 offset:20992\n\t"
    "v_fma_f32 v28, v68, s56, v56\n\t"
    "v_fma_f32 v29, v69, s56, v57\n\t"
    "s_waitcnt lgkmcnt(7)\n\t"
    "v_mfma_f32_16x16x32_bf16 v[60:63], v[84:87], v[128:131], v[60:63]\n\t"
    "s_waitcnt vmcnt(4) lgkmcnt(4)\n\t"
    "s_barrier\n\t"
    "ds_read_b64_tr_b16 v[76:77], v20 offset:24576\n\t"
    "v_fma_f32 v30, v70, s56, v58\n\t"
    "v_fma_f32 v31, v71, s56, v59\n\t"
    "v_mfma_f32_16x16x32_bf16 v[64:67], v[84:87], v[132:135], v[64:67]\n\t"
    "ds_read_b64_tr_b16 v[78:79], v20 offset:25088\n\t"
    "v_exp_f32_e32 v36, v28\n\t"
    "v_exp_f32_e32 v37, v29\n\t"
    "s_add_u32 m0, s57, 0x0\n\t"
    "s_add_u32 s54, s53, 0x100\n\t"
    "buffer_load_dwordx4 v21, s[40:43], s54 offen  lds\n\t"
    "v_mfma_f32_16x16x32_bf16 v[60:63], v[88:91], v[136:139], v[60:63]\n\t"
    "ds_read_b64_tr_b16 v[80:81], v20 offset:28672\n\t"
    "v_exp_f32_e32 v38, v30\n\t"
    "v_mfma_f32_16x16x32_bf16 v[64:67], v[88:91], v[140:143], v[64:67]\n\t"
    "ds_read_b64_tr_b16 v[82:83], v20 offset:29184\n\t"
    "v_exp_f32_e32 v39, v31\n\t"
    "s_waitcnt lgkmcnt(6)\n\t"
    "v_mfma_f32_16x16x32_bf16 v[60:63], v[92:95], v[144:147], v[60:63]\n\t"
    "ds_read_b64_tr_b16 v[84:85], v104 offset:0\n\t"
    "v_fma_f32 v44, v36, 0.5, 1.0\n\t"
    "v_fma_f32 v45, v37, 0.5, 1.0\n\t"
    "v_mfma_f32_16x16x32_bf16 v[64:67], v[92:95], v[148:151], v[64:67]\n\t"
    "ds_read_b64_tr_b16 v[86:87], v104 offset:512\n\t"
    "v_fma_f32 v46, v38, 0.5, 1.0\n\t"
    "v_fma_f32 v47, v39, 0.5, 1.0\n\t"
    "s_add_u32 m0, s57, 0x2000\n\t"
    "s_add_u32 s54, s53, 0x90100\n\t"
    "buffer_load_dwordx4 v21, s[40:43], s54 offen  lds\n\t"
    "s_waitcnt lgkmcnt(6)\n\t"
    "v_mfma_f32_16x16x32_bf16 v[60:63], v[96:99], v[152:155], v[60:63]\n\t"
    "ds_read_b64_tr_b16 v[88:89], v104 offset:4096\n\t"
    "v_fma_f32 v44, v36, v44, 1.0\n\t"
    "v_fma_f32 v45, v37, v45, 1.0\n\t"
    "v_fma_f32 v46, v38, v46, 1.0\n\t"
    "v_mfma_f32_16x16x32_bf16 v[64:67], v[96:99], v[156:159], v[64:67]\n\t"
    "ds_read_b64_tr_b16 v[90:91], v104 offset:4608\n\t"
    "v_fma_f32 v47, v39, v47, 1.0\n\t"
    "v_rcp_f32_e32 v44, v44\n\t"
    "s_waitcnt lgkmcnt(6)\n\t"
    "v_mfma_f32_16x16x32_bf16 v[60:63], v[76:79], v[160:163], v[60:63]\n\t"
    "s_waitcnt vmcnt(4) lgkmcnt(4)\n\t"
    "s_barrier\n\t"
    "ds_read_b64_tr_b16 v[92:93], v104 offset:8192\n\t"
    "v_rcp_f32_e32 v45, v45\n\t"
    "v_mfma_f32_16x16x32_bf16 v[64:67], v[76:79], v[164:167], v[64:67]\n\t"
    "ds_read_b64_tr_b16 v[94:95], v104 offset:8704\n\t"
    "v_rcp_f32_e32 v46, v46\n\t"
    "s_add_u32 m0, s57, 0x4000\n\t"
    "s_add_u32 s54, s53, 0x120100\n\t"
    "buffer_load_dwordx4 v21, s[40:43], s54 offen  lds\n\t"
    "v_mfma_f32_16x16x32_bf16 v[60:63], v[80:83], v[168:171], v[60:63]\n\t"
    "ds_read_b64_tr_b16 v[96:97], v104 offset:12288\n\t"
    "v_rcp_f32_e32 v47, v47\n\t"
    "v_mfma_f32_16x16x32_bf16 v[64:67], v[80:83], v[172:175], v[64:67]\n\t"
    "ds_read_b64_tr_b16 v[98:99], v104 offset:12800\n\t"
    "v_fma_f32 v44, -v28, v44, v28\n\t"
    "v_fma_f32 v45, -v29, v45, v29\n\t"
    "v_fma_f32 v46, -v30, v46, v30\n\t"
    "s_waitcnt lgkmcnt(6)\n\t"
    "v_mfma_f32_16x16x32_bf16 v[60:63], v[84:87], v[176:179], v[60:63]\n\t"
    "ds_read_b64_tr_b16 v[76:77], v104 offset:16384\n\t"
    "v_fma_f32 v47, -v31, v47, v31\n\t"
    "v_cvt_pk_bf16_f32 v52, v44, v45\n\t"
    "v_mfma_f32_16x16x32_bf16 v[64:67], v[84:87], v[180:183], v[64:67]\n\t"
    "ds_read_b64_tr_b16 v[78:79], v104 offset:16896\n\t"
    "v_cvt_pk_bf16_f32 v53, v46, v47\n\t"
    "ds_write_b64 v26, v[52:53] offset:0\n\t"
    "s_add_u32 m0, s57, 0x6000\n\t"
    "s_add_u32 s54, s53, 0x1b0100\n\t"
    "buffer_load_dwordx4 v21, s[40:43], s54 offen  lds\n\t"
    "s_waitcnt lgkmcnt(7)\n\t"
    "v_mfma_f32_16x16x32_bf16 v[60:63], v[88:91], v[184:187], v[60:63]\n\t"
    "ds_read_b64_tr_b16 v[80:81], v104 offset:20480\n\t"
    "v_fma_f32 v32, v72, s56, v56\n\t"
    "v_fma_f32 v33, v73, s56, v57\n\t"
    "v_fma_f32 v34, v74, s56, v58\n\t"
    "v_mfma_f32_16x16x32_bf16 v[64:67], v[88:91], v[188:191], v[64:67]\n\t"
    "ds_read_b64_tr_b16 v[82:83], v104 offset:20992\n\t"
    "v_fma_f32 v35, v75, s56, v59\n\t"
    "v_exp_f32_e32 v40, v32\n\t"
    "s_waitcnt lgkmcnt(7)\n\t"
    "v_mfma_f32_16x16x32_bf16 v[60:63], v[92:95], v[192:195], v[60:63]\n\t"
    "s_waitcnt vmcnt(4) lgkmcnt(5)\n\t"
    "s_barrier\n\t"
    "ds_read_b64_tr_b16 v[84:85], v104 offset:24576\n\t"
    "v_exp_f32_e32 v41, v33\n\t"
    "v_mfma_f32_16x16x32_bf16 v[64:67], v[92:95], v[196:199], v[64:67]\n\t"
    "ds_read_b64_tr_b16 v[86:87], v104 offset:25088\n\t"
    "v_exp_f32_e32 v42, v34\n\t"
    "s_add_u32 m0, s57, 0x8000\n\t"
    "s_add_u32 s54, s53, 0x240100\n\t"
    "buffer_load_dwordx4 v21, s[40:43], s54 offen  lds\n\t"
    "v_mfma_f32_16x16x32_bf16 v[60:63], v[96:99], v[200:203], v[60:63]\n\t"
    "ds_read_b64_tr_b16 v[88:89], v104 offset:28672\n\t"
    "v_exp_f32_e32 v43, v35\n\t"
    "v_mfma_f32_16x16x32_bf16 v[64:67], v[96:99], v[204:207], v[64:67]\n\t"
    "ds_read_b64_tr_b16 v[90:91], v104 offset:29184\n\t"
    "v_fma_f32 v48, v40, 0.5, 1.0\n\t"
    "v_fma_f32 v49, v41, 0.5, 1.0\n\t"
    "s_waitcnt lgkmcnt(7)\n\t"
    "v_mfma_f32_16x16x32_bf16 v[60:63], v[76:79], v[208:211], v[60:63]\n\t"
    "ds_read_b64_tr_b16 v[92:93], v104 offset:32768\n\t"
    "ds_read_b128 v[56:59], v27\n\t"
    "v_add_u32_e32 v27, 0x100, v27\n\t"
    "v_fma_f32 v50, v42, 0.5, 1.0\n\t"
    "v_mfma_f32_16x16x32_bf16 v[64:67], v[76:79], v[212:215], v[64:67]\n\t"
    "ds_read_b64_tr_b16 v[94:95], v104 offset:33280\n\t"
    "v_fma_f32 v51, v43, 0.5, 1.0\n\t"
    "v_fma_f32 v48, v40, v48, 1.0\n\t"
    "s_add_u32 m0, s57, 0xa000\n\t"
    "s_add_u32 s54, s53, 0x2d0100\n\t"
    "buffer_load_dwordx4 v21, s[40:43], s54 offen  lds\n\t"
    "s_waitcnt lgkmcnt(7)\n\t"
    "v_mfma_f32_16x16x32_bf16 v[60:63], v[80:83], v[216:219], v[60:63]\n\t"
    "ds_read_b64_tr_b16 v[96:97], v104 offset:36864\n\t"
    "v_fma_f32 v49, v41, v49, 1.0\n\t"
    "v_fma_f32 v50, v42, v50, 1.0\n\t"
    "v_fma_f32 v51, v43, v51, 1.0\n\t"
    "v_mfma_f32_16x16x32_bf16 v[64:67], v[80:83], v[220:223], v[64:67]\n\t"
    "ds_read_b64_tr_b16 v[98:99], v104 offset:37376\n\t"
    "v_rcp_f32_e32 v48, v48\n\t"
    "s_waitcnt lgkmcnt(7)\n\t"
    "v_mfma_f32_16x16x32_bf16 v[60:63], v[84:87], v[224:227], v[60:63]\n\t"
    "s_waitcnt vmcnt(4) lgkmcnt(5)\n\t"
    "s_barrier\n\t"
    "ds_read_b64_tr_b16 v[76:77], v20 offset:0\n\t"
    "v_rcp_f32_e32 v49, v49\n\t"
    "v_mfma_f32_16x16x32_bf16 v[64:67], v[84:87], v[228:231], v[64:67]\n\t"
    "ds_read_b64_tr_b16 v[78:79], v20 offset:512\n\t"
    "v_rcp_f32_e32 v50, v50\n\t"
    "s_add_u32 m0, s57, 0xc000\n\t"
    "s_add_u32 s54, s53, 0x360100\n\t"
    "buffer_load_dwordx4 v21, s[40:43], s54 offen  lds\n\t"
    "v_mfma_f32_16x16x32_bf16 v[60:63], v[88:91], v[232:235], v[60:63]\n\t"
    "ds_read_b64_tr_b16 v[80:81], v20 offset:4096\n\t"
    "v_rcp_f32_e32 v51, v51\n\t"
    "v_fma_f32 v48, -v32, v48, v32\n\t"
    "v_mfma_f32_16x16x32_bf16 v[64:67], v[88:91], v[236:239], v[64:67]\n\t"
    "ds_read_b64_tr_b16 v[82:83], v20 offset:4608\n\t"
    "v_fma_f32 v49, -v33, v49, v33\n\t"
    "v_fma_f32 v50, -v34, v50, v34\n\t"
    "s_waitcnt lgkmcnt(6)\n\t"
    "v_mfma_f32_16x16x32_bf16 v[60:63], v[92:95], v[240:243], v[60:63]\n\t"
    "ds_read_b64_tr_b16 v[84:85], v20 offset:8192\n\t"
    "v_fma_f32 v51, -v35, v51, v35\n\t"
    "v_cvt_pk_bf16_f32 v54, v48, v49\n\t"
    "v_cvt_pk_bf16_f32 v55, v50, v51\n\t"
    "v_mfma_f32_16x16x32_bf16 v[64:67], v[92:95], v[244:247], v[64:67]\n\t"
    "ds_read_b64_tr_b16 v[86:87], v20 offset:8704\n\t"
    "ds_write_b64 v26, v[54:55] offset:2048\n\t"
    "v_add_u32_e32 v26, s58, v26\n\t"
    "s_movk_i32 s58, 0x2000\n\t"
    "s_add_u32 m0, s57, 0xe000\n\t"
    "s_add_u32 s54, s53, 0x3f0100\n\t"
    "buffer_load_dwordx4 v21, s[40:43], s54 offen  lds\n\t"
    "s_waitcnt lgkmcnt(7)\n\t"
    "v_mfma_f32_16x16x32_bf16 v[68:71], v[96:99], v[248:251], v[60:63]\n\t"
    "ds_read_b64_tr_b16 v[88:89], v20 offset:12288\n\t"
    "v_mfma_f32_16x16x32_bf16 v[72:75], v[96:99], v[252:255], v[64:67]\n\t"
    "ds_read_b64_tr_b16 v[90:91], v20 offset:12800\n\t"
    "s_add_u32 s53, s53, 0x100\n\t"
    "s_sub_u32 s55, s55, 1\n\t"
    "s_cmp_lg_u32 s55, 0\n\t"
    "s_cbranch_scc1 LA_%=\n\t"
    "s_waitcnt lgkmcnt(7)\n\t"
    "v_mfma_f32_16x16x32_bf16 v[60:63], v[76:79], v[112:115], 0\n\t"
    "s_waitcnt vmcnt(4) lgkmcnt(5)\n\t"
    "s_barrier\n\t"
    "ds_read_b64_tr_b16 v[92:93], v20 offset:16384\n\t"
    "v_mfma_f32_16x16x32_bf16 v[64:67], v[76:79], v[116:119], 0\n\t"
    "ds_read_b64_tr_b16 v[94:95], v20 offset:16896\n\t"
    "s_sub_u32 s83, s83, 1\n\t"
    "s_cmp_eq_u32 s83, 0\n\t"
    "s_cbranch_scc0 LrB_%=\n\t"
    "global_load_dword v101, v[102:103], off \n\t"
    "LrB_%=:\n\t"
    "s_add_u32 m0, s57, 0x10000\n\t"
    "s_mov_b32 s54, 0x480400\n\t"
    "buffer_load_dwordx4 v21, s[40:43], s54 offen  lds\n\t"
    "ds_read_b128 v[112:115], v18 offset:0\n\t"
    "ds_read_b128 v[116:119], v18 offset:2048\n\t"
    "v_mfma_f32_16x16x32_bf16 v[60:63], v[80:83], v[120:123], v[60:63]\n\t"
    "ds_read_b64_tr_b16 v[96:97], v20 offset:20480\n\t"
    "v_mfma_f32_16x16x32_bf16 v[64:67], v[80:83], v[124:127], v[64:67]\n\t"
    "ds_read_b64_tr_b16 v[98:99], v20 offset:20992\n\t"
    "v_fma_f32 v28, v68, s56, v56\n\t"
    "v_fma_f32 v29, v69, s56, v57\n\t"
    "v_fma_f32 v30, v70, s56, v58\n\t"
    "ds_read_b128 v[120:123], v19 offset:0\n\t"
    "ds_read_b128 v[124:127], v19 offset:2048\n\t"
    "s_waitcnt lgkmcnt(11)\n\t"
    "v_mfma_f32_16x16x32_bf16 v[60:63], v[84:87], v[128:131], v[60:63]\n\t"
    "ds_read_b64_tr_b16 v[76:77], v20 offset:24576\n\t"
    "v_fma_f32 v31, v71, s56, v59\n\t"
    "v_exp_f32_e32 v36, v28\n\t"
    "v_mfma_f32_16x16x32_bf16 v[64:67], v[84:87], v[132:135], v[64:67]\n\t"
    "ds_read_b64_tr_b16 v[78:79], v20 offset:25088\n\t"
    "v_exp_f32_e32 v37, v29\n\t"
    "v_exp_f32_e32 v38, v30\n\t"
    "s_add_u32 m0, s57, 0x0\n\t"
    "s_mov_b32 s54, 0x0\n\t"
    "buffer_load_dwordx4 v22, s[44:47], s54 offen  lds\n\t"
    "ds_read_b128 v[128:131], v18 offset:8192\n\t"
    "ds_read_b128 v[132:135], v18 offset:10240\n\t"
    "s_waitcnt lgkmcnt(12)\n\t"
    "v_mfma_f32_16x16x32_bf16 v[60:63], v[88:91], v[136:139], v[60:63]\n\t"
    "ds_read_b64_tr_b16 v[80:81], v20 offset:28672\n\t"
    "v_exp_f32_e32 v39, v31\n\t"
    "v_mfma_f32_16x16x32_bf16 v[64:67], v[88:91], v[140:143], v[64:67]\n\t"
    "ds_read_b64_tr_b16 v[82:83], v20 offset:29184\n\t"
    "v_fma_f32 v44, v36, 0.5, 1.0\n\t"
    "v_fma_f32 v45, v37, 0.5, 1.0\n\t"
    "v_fma_f32 v46, v38, 0.5, 1.0\n\t"
    "ds_read_b128 v[136:139], v19 offset:8192\n\t"
    "ds_read_b128 v[140:143], v19 offset:10240\n\t"
    "s_waitcnt lgkmcnt(14)\n\t"
    "v_mfma_f32_16x16x32_bf16 v[60:63], v[92:95], v[144:147], v[60:63]\n\t"
    "s_waitcnt vmcnt(4) lgkmcnt(10)\n\t"
    "s_barrier\n\t"
    "ds_read_b64_tr_b16 v[84:85], v104 offset:0\n\t"
    "v_fma_f32 v47, v39, 0.5, 1.0\n\t"
    "v_fma_f32 v44, v36, v44, 1.0\n\t"
    "v_fma_f32 v45, v37, v45, 1.0\n\t"
    "v_mfma_f32_16x16x32_bf16 v[64:67], v[92:95], v[148:151], v[64:67]\n\t"
    "ds_read_b64_tr_b16 v[86:87], v104 offset:512\n\t"
    "v_fma_f32 v46, v38, v46, 1.0\n\t"
    "v_fma_f32 v47, v39, v47, 1.0\n\t"
    "v_rcp_f32_e32 v44, v44\n\t"
    "s_add_u32 m0, s57, 0x2000\n\t"
    "s_mov_b32 s54, 0x12000\n\t"
    "buffer_load_dwordx4 v22, s[44:47], s54 offen  lds\n\t"
    "ds_read_b128 v[144:147], v18 offset:16384\n\t"
    "ds_read_b128 v[148:151], v18 offset:18432\n\t"
    "v_mfma_f32_16x16x32_bf16 v[60:63], v[96:99], v[152:155], v[60:63]\n\t"
    "ds_read_b64_tr_b16 v[88:89], v104 offset:4096\n\t"
    "v_rcp_f32_e32 v45, v45\n\t"
    "v_mfma_f32_16x16x32_bf16 v[64:67], v[96:99], v[156:159], v[64:67]\n\t"
    "ds_read_b64_tr_b16 v[90:91], v104 offset:4608\n\t"
    "v_rcp_f32_e32 v46, v46\n\t"
    "v_rcp_f32_e32 v47, v47\n\t"
    "ds_read_b128 v[152:155], v19 offset:16384\n\t"
    "ds_read_b128 v[156:159], v19 offset:18432\n\t"
    "s_waitcnt lgkmcnt(14)\n\t"
    "v_mfma_f32_16x16x32_bf16 v[60:63], v[76:79], v[160:163], v[60:63]\n\t"
    "ds_read_b64_tr_b16 v[92:93], v104 offset:8192\n\t"
    "v_fma_f32 v44, -v28, v44, v28\n\t"
    "v_fma_f32 v45, -v29, v45, v29\n\t"
    "v_mfma_f32_16x16x32_bf16 v[64:67], v[76:79], v[164:167], v[64:67]\n\t"
    "ds_read_b64_tr_b16 v[94:95], v104 offset:8704\n\t"
    "v_fma_f32 v46, -v30, v46, v30\n\t"
    "v_fma_f32 v47, -v31, v47, v31\n\t"
    "v_cvt_pk_bf16_f32 v52, v44, v45\n\t"
    "s_add_u32 m0, s57, 0x4000\n\t"
    "s_mov_b32 s54, 0x24000\n\t"
    "buffer_load_dwordx4 v22, s[44:47], s54 offen  lds\n\t"
    "ds_read_b128 v[160:163], v18 offset:24576\n\t"
    "ds_read_b128 v[164:167], v18 offset:26624\n\t"
    "s_waitcnt lgkmcnt(14)\n\t"
    "v_mfma_f32_16x16x32_bf16 v[60:63], v[80:83], v[168:171], v[60:63]\n\t"
    "ds_read_b64_tr_b16 v[96:97], v104 offset:12288\n\t"
    "v_cvt_pk_bf16_f32 v53, v46, v47\n\t"
    "ds_write_b64 v26, v[52:53] offset:0\n\t"
    "v_fma_f32 v32, v72, s56, v56\n\t"
    "v_fma_f32 v33, v73, s56, v57\n\t"
    "v_mfma_f32_16x16x32_bf16 v[64:67], v[80:83], v[172:175], v[64:67]\n\t"
    "ds_read_b64_tr_b16 v[98:99], v104 offset:12800\n\t"
    "v_fma_f32 v34, v74, s56, v58\n\t"
    "v_fma_f32 v35, v75, s56, v59\n\t"
    "ds_read_b128 v[168:171], v19 offset:24576\n\t"
    "ds_read_b128 v[172:175], v19 offset:26624\n\t"
    "s_waitcnt lgkmcnt(15)\n\t"
    "v_mfma_f32_16x16x32_bf16 v[60:63], v[84:87], v[176:179], v[60:63]\n\t"
    "s_waitcnt vmcnt(4) lgkmcnt(11)\n\t"
    "s_barrier\n\t"
    "ds_read_b64_tr_b16 v[76:77], v104 offset:16384\n\t"
    "v_exp_f32_e32 v40, v32\n\t"
    "v_exp_f32_e32 v41, v33\n\t"
    "v_mfma_f32_16x16x32_bf16 v[64:67], v[84:87], v[180:183], v[64:67]\n\t"
    "ds_read_b64_tr_b16 v[78:79], v104 offset:16896\n\t"
    "v_exp_f32_e32 v42, v34\n\t"
    "s_add_u32 m0, s57, 0x6000\n\t"
    "s_mov_b32 s54, 0x36000\n\t"
    "buffer_load_dwordx4 v22, s[44:47], s54 offen  lds\n\t"
    "ds_read_b128 v[176:179], v105 offset:0\n\t"
    "ds_read_b128 v[180:183], v105 offset:2048\n\t"
    "v_mfma_f32_16x16x32_bf16 v[60:63], v[88:91], v[184:187], v[60:63]\n\t"
    "ds_read_b64_tr_b16 v[80:81], v104 offset:20480\n\t"
    "v_exp_f32_e32 v43, v35\n\t"
    "v_fma_f32 v48, v40, 0.5, 1.0\n\t"
    "v_fma_f32 v49, v41, 0.5, 1.0\n\t"
    "v_mfma_f32_16x16x32_bf16 v[64:67], v[88:91], v[188:191], v[64:67]\n\t"
    "ds_read_b64_tr_b16 v[82:83], v104 offset:20992\n\t"
    "ds_read_b128 v[56:59], v27\n\t"
    "v_add_u32_e32 v27, 0x100, v27\n\t"
    "v_fma_f32 v50, v42, 0.5, 1.0\n\t"
    "ds_read_b128 v[184:187], v106 offset:0\n\t"
    "ds_read_b128 v[188:191], v106 offset:2048\n\t"
    "s_waitcnt lgkmcnt(15)\n\t"
    "v_mfma_f32_16x16x32_bf16 v[60:63], v[92:95], v[192:195], v[60:63]\n\t"
    "ds_read_b64_tr_b16 v[84:85], v104 offset:24576\n\t"
    "v_fma_f32 v51, v43, 0.5, 1.0\n\t"
    "v_fma_f32 v48, v40, v48, 1.0\n\t"
    "v_fma_f32 v49, v41, v49, 1.0\n\t"
    "v_mfma_f32_16x16x32_bf16 v[64:67], v[92:95], v[196:199], v[64:67]\n\t"
    "ds_read_b64_tr_b16 v[86:87], v104 offset:25088\n\t"
    "v_fma_f32 v50, v42, v50, 1.0\n\t"
    "v_fma_f32 v51, v43, v51, 1.0\n\t"
    "s_add_u32 m0, s57, 0x8000\n\t"
    "s_mov_b32 s54, 0x48000\n\t"
    "buffer_load_dwordx4 v22, s[44:47], s54 offen  lds\n\t"
    "ds_read_b128 v[192:195], v105 offset:8192\n\t"
    "ds_read_b128 v[196:199], v105 offset:10240\n\t"
    "s_waitcnt lgkmcnt(15)\n\t"
    "v_mfma_f32_16x16x32_bf16 v[60:63], v[96:99], v[200:203], v[60:63]\n\t"
    "ds_read_b64_tr_b16 v[88:89], v104 offset:28672\n\t"
    "v_rcp_f32_e32 v48, v48\n\t"
    "v_rcp_f32_e32 v49, v49\n\t"
    "v_mfma_f32_16x16x32_bf16 v[64:67], v[96:99], v[204:207], v[64:67]\n\t"
    "ds_read_b64_tr_b16 v[90:91], v104 offset:29184\n\t"
    "v_rcp_f32_e32 v50, v50\n\t"
    "ds_read_b128 v[200:203], v106 offset:8192\n\t"
    "ds_read_b128 v[204:207], v106 offset:10240\n\t"
    "s_waitcnt lgkmcnt(15)\n\t"
    "v_mfma_f32_16x16x32_bf16 v[60:63], v[76:79], v[208:211], v[60:63]\n\t"
    "s_waitcnt vmcnt(4) lgkmcnt(11)\n\t"
    "s_barrier\n\t"
    "ds_read_b64_tr_b16 v[92:93], v104 offset:32768\n\t"
    "v_rcp_f32_e32 v51, v51\n\t"
    "v_fma_f32 v48, -v32, v48, v32\n\t"
    "v_fma_f32 v49, -v33, v49, v33\n\t"
    "v_mfma_f32_16x16x32_bf16 v[64:67], v[76:79], v[212:215], v[64:67]\n\t"
    "ds_read_b64_tr_b16 v[94:95], v104 offset:33280\n\t"
    "v_fma_f32 v50, -v34, v50, v34\n\t"
    "v_fma_f32 v51, -v35, v51, v35\n\t"
    "v_cvt_pk_bf16_f32 v54, v48, v49\n\t"
    "s_add_u32 m0, s57, 0xa000\n\t"
    "s_mov_b32 s54, 0x5a000\n\t"
    "buffer_load_dwordx4 v22, s[44:47], s54 offen  lds\n\t"
    "ds_read_b128 v[208:211], v105 offset:16384\n\t"
    "ds_read_b128 v[212:215], v105 offset:18432\n\t"
    "v_mfma_f32_16x16x32_bf16 v[60:63], v[80:83], v[216:219], v[60:63]\n\t"
    "ds_read_b64_tr_b16 v[96:97], v104 offset:36864\n\t"
    "v_cvt_pk_bf16_f32 v55, v50, v51\n\t"
    "ds_write_b64 v26, v[54:55] offset:2048\n\t"
    "v_add_u32_e32 v26, s58, v26\n\t"
    "s_movk_i32 s58, 0x2000\n\t"
    "v_mfma_f32_16x16x32_bf16 v[64:67], v[80:83], v[220:223], v[64:67]\n\t"
    "ds_read_b64_tr_b16 v[98:99], v104 offset:37376\n\t"
    "ds_read_b128 v[216:219], v106 offset:16384\n\t"
    "ds_read_b128 v[220:223], v106 offset:18432\n\t"
    "s_waitcnt lgkmcnt(15)\n\t"
    "v_mfma_f32_16x16x32_bf16 v[60:63], v[84:87], v[224:227], v[60:63]\n\t"
    "ds_read_b64_tr_b16 v[76:77], v20 offset:0\n\t"
    "v_mfma_f32_16x16x32_bf16 v[64:67], v[84:87], v[228:231], v[64:67]\n\t"
    "ds_read_b64_tr_b16 v[78:79], v20 offset:512\n\t"
    "s_add_u32 m0, s57, 0xc000\n\t"
    "s_mov_b32 s54, 0x6c000\n\t"
    "buffer_load_dwordx4 v22, s[44:47], s54 offen  lds\n\t"
    "s_waitcnt lgkmcnt(13)\n\t"
    "v_mfma_f32_16x16x32_bf16 v[60:63], v[88:91], v[232:235], v[60:63]\n\t"
    "ds_read_b64_tr_b16 v[80:81], v20 offset:4096\n\t"
    "v_mfma_f32_16x16x32_bf16 v[64:67], v[88:91], v[236:239], v[64:67]\n\t"
    "ds_read_b64_tr_b16 v[82:83], v20 offset:4608\n\t"
    "s_waitcnt lgkmcnt(11)\n\t"
    "v_mfma_f32_16x16x32_bf16 v[60:63], v[92:95], v[240:243], v[60:63]\n\t"
    "s_waitcnt vmcnt(4) lgkmcnt(6)\n\t"
    "s_barrier\n\t"
    "ds_read_b64_tr_b16 v[84:85], v20 offset:8192\n\t"
    "v_mfma_f32_16x16x32_bf16 v[64:67], v[92:95], v[244:247], v[64:67]\n\t"
    "ds_read_b64_tr_b16 v[86:87], v20 offset:8704\n\t"
    "s_add_u32 m0, s57, 0xe000\n\t"
    "s_mov_b32 s54, 0x7e000\n\t"
    "buffer_load_dwordx4 v22, s[44:47], s54 offen  lds\n\t"
    "ds_read_b128 v[224:227], v105 offset:24576\n\t"
    "ds_read_b128 v[228:231], v105 offset:26624\n\t"
    "v_mfma_f32_16x16x32_bf16 v[68:71], v[96:99], v[248:251], v[60:63]\n\t"
    "ds_read_b64_tr_b16 v[88:89], v20 offset:12288\n\t"
    "v_mfma_f32_16x16x32_bf16 v[72:75], v[96:99], v[252:255], v[64:67]\n\t"
    "ds_read_b64_tr_b16 v[90:91], v20 offset:12800\n\t"
    "ds_read_b128 v[232:235], v106 offset:24576\n\t"
    "ds_read_b128 v[236:239], v106 offset:26624\n\t"
    "s_waitcnt lgkmcnt(10)\n\t"
    "v_mfma_f32_16x16x32_bf16 v[60:63], v[76:79], v[112:115], 0\n\t"
    "ds_read_b64_tr_b16 v[92:93], v20 offset:16384\n\t"
    "v_mfma_f32_16x16x32_bf16 v[64:67], v[76:79], v[116:119], 0\n\t"
    "ds_read_b64_tr_b16 v[94:95], v20 offset:16896\n\t"
    "s_add_u32 m0, s57, 0x10000\n\t"
    "s_mov_b32 s54, 0x90000\n\t"
    "buffer_load_dwordx4 v22, s[44:47], s54 offen  lds\n\t"
    "s_waitcnt lgkmcnt(10)\n\t"
    "v_mfma_f32_16x16x32_bf16 v[60:63], v[80:83], v[120:123], v[60:63]\n\t"
    "ds_read_b64_tr_b16 v[96:97], v20 offset:20480\n\t"
    "v_mfma_f32_16x16x32_bf16 v[64:67], v[80:83], v[124:127], v[64:67]\n\t"
    "ds_read_b64_tr_b16 v[98:99], v20 offset:20992\n\t"
    "v_fma_f32 v28, v68, s56, v56\n\t"
    "v_fma_f32 v29, v69, s56, v57\n\t"
    "v_fma_f32 v30, v70, s56, v58\n\t"
    "v_fma_f32 v31, v71, s56, v59\n\t"
    "v_exp_f32_e32 v36, v28\n\t"
    "s_waitcnt lgkmcnt(10)\n\t"
    "v_mfma_f32_16x16x32_bf16 v[60:63], v[84:87], v[128:131], v[60:63]\n\t"
    "s_waitcnt vmcnt(4) lgkmcnt(6)\n\t"
    "s_barrier\n\t"
    "ds_read_b64_tr_b16 v[76:77], v20 offset:24576\n\t"
    "v_exp_f32_e32 v37, v29\n\t"
    "v_exp_f32_e32 v38, v30\n\t"
    "v_exp_f32_e32 v39, v31\n\t"
    "v_mfma_f32_16x16x32_bf16 v[64:67], v[84:87], v[132:135], v[64:67]\n\t"
    "ds_read_b64_tr_b16 v[78:79], v20 offset:25088\n\t"
    "v_fma_f32 v44, v36, 0.5, 1.0\n\t"
    "v_fma_f32 v45, v37, 0.5, 1.0\n\t"
    "v_fma_f32 v46, v38, 0.5, 1.0\n\t"
    "v_fma_f32 v47, v39, 0.5, 1.0\n\t"
    "v_fma_f32 v44, v36, v44, 1.0\n\t"
    "v_fma_f32 v45, v37, v45, 1.0\n\t"
    "s_add_u32 m0, s57, 0x0\n\t"
    "s_mov_b32 s54, 0x80\n\t"
    "buffer_load_dwordx4 v22, s[44:47], s54 offen  lds\n\t"
    "v_mfma_f32_16x16x32_bf16 v[60:63], v[88:91], v[136:139], v[60:63]\n\t"
    "ds_read_b64_tr_b16 v[80:81], v20 offset:28672\n\t"
    "v_fma_f32 v46, v38, v46, 1.0\n\t"
    "v_fma_f32 v47, v39, v47, 1.0\n\t"
    "v_rcp_f32_e32 v44, v44\n\t"
    "v_rcp_f32_e32 v45, v45\n\t"
    "v_mfma_f32_16x16x32_bf16 v[64:67], v[88:91], v[140:143], v[64:67]\n\t"
    "ds_read_b64_tr_b16 v[82:83], v20 offset:29184\n\t"
    "v_rcp_f32_e32 v46, v46\n\t"
    "v_rcp_f32_e32 v47, v47\n\t"
    "v_fma_f32 v44, -v28, v44, v28\n\t"
    "v_fma_f32 v45, -v29, v45, v29\n\t"
    "s_waitcnt lgkmcnt(6)\n\t"
    "v_mfma_f32_16x16x32_bf16 v[60:63], v[92:95], v[144:147], v[60:63]\n\t"
    "ds_read_b64_tr_b16 v[84:85], v104 offset:0\n\t"
    "v_fma_f32 v46, -v30, v46, v30\n\t"
    "v_fma_f32 v47, -v31, v47, v31\n\t"
    "v_cvt_pk_bf16_f32 v52, v44, v45\n\t"
    "v_cvt_pk_bf16_f32 v53, v46, v47\n\t"
    "ds_write_b64 v26, v[52:53] offset:0\n\t"
    "v_fma_f32 v32, v72, s56, v56\n\t"
    "v_mfma_f32_16x16x32_bf16 v[64:67], v[92:95], v[148:151], v[64:67]\n\t"
    "ds_read_b64_tr_b16 v[86:87], v104 offset:512\n\t"
    "v_fma_f32 v33, v73, s56, v57\n\t"
    "v_fma_f32 v34, v74, s56, v58\n\t"
    "v_fma_f32 v35, v75, s56, v59\n\t"
    "v_exp_f32_e32 v40, v32\n\t"
    "s_add_u32 m0, s57, 0x2000\n\t"
    "s_mov_b32 s54, 0x12080\n\t"
    "buffer_load_dwordx4 v22, s[44:47], s54 offen  lds\n\t"
    "s_waitcnt lgkmcnt(7)\n\t"
    "v_mfma_f32_16x16x32_bf16 v[60:63], v[96:99], v[152:155], v[60:63]\n\t"
    "ds_read_b64_tr_b16 v[88:89], v104 offset:4096\n\t"
    "v_exp_f32_e32 v41, v33\n\t"
    "v_exp_f32_e32 v42, v34\n\t"
    "v_exp_f32_e32 v43, v35\n\t"
    "v_mfma_f32_16x16x32_bf16 v[64:67], v[96:99], v[156:159], v[64:67]\n\t"
    "ds_read_b64_tr_b16 v[90:91], v104 offset:4608\n\t"
    "v_fma_f32 v48, v40, 0.5, 1.0\n\t"
    "v_fma_f32 v49, v41, 0.5, 1.0\n\t"
    "v_fma_f32 v50, v42, 0.5, 1.0\n\t"
    "v_fma_f32 v51, v43, 0.5, 1.0\n\t"
    "v_fma_f32 v48, v40, v48, 1.0\n\t"
    "v_fma_f32 v49, v41, v49, 1.0\n\t"
    "s_waitcnt lgkmcnt(7)\n\t"
    "v_mfma_f32_16x16x32_bf16 v[60:63], v[76:79], v[160:163], v[60:63]\n\t"
    "s_waitcnt vmcnt(4) lgkmcnt(5)\n\t"
    "s_barrier\n\t"
    "ds_read_b64_tr_b16 v[92:93], v104 offset:8192\n\t"
    "v_fma_f32 v50, v42, v50, 1.0\n\t"
    "v_fma_f32 v51, v43, v51, 1.0\n\t"
    "v_rcp_f32_e32 v48, v48\n\t"
    "v_rcp_f32_e32 v49, v49\n\t"
    "v_mfma_f32_16x16x32_bf16 v[64:67], v[76:79], v[164:167], v[64:67]\n\t"
    "ds_read_b64_tr_b16 v[94:95], v104 offset:8704\n\t"
    "v_rcp_f32_e32 v50, v50\n\t"
    "v_rcp_f32_e32 v51, v51\n\t"
    "v_fma_f32 v48, -v32, v48, v32\n\t"
    "v_fma_f32 v49, -v33, v49, v33\n\t"
    "s_add_u32 m0, s57, 0x4000\n\t"
    "s_mov_b32 s54, 0x24080\n\t"
    "buffer_load_dwordx4 v22, s[44:47], s54 offen  lds\n\t"
    "v_mfma_f32_16x16x32_bf16 v[60:63], v[80:83], v[168:171], v[60:63]\n\t"
    "ds_read_b64_tr_b16 v[96:97], v104 offset:12288\n\t"
    "v_fma_f32 v50, -v34, v50, v34\n\t"
    "v_fma_f32 v51, -v35, v51, v35\n\t"
    "v_cvt_pk_bf16_f32 v54, v48, v49\n\t"
    "v_cvt_pk_bf16_f32 v55, v50, v51\n\t"
    "ds_write_b64 v26, v[54:55] offset:2048\n\t"
    "v_add_u32_e32 v26, s58, v26\n\t"
    "s_movk_i32 s58, 0x2000\n\t"
    "v_mfma_f32_16x16x32_bf16 v[64:67], v[80:83], v[172:175], v[64:67]\n\t"
    "ds_read_b64_tr_b16 v[98:99], v104 offset:12800\n\t"
    "s_waitcnt lgkmcnt(7)\n\t"
    "v_mfma_f32_16x16x32_bf16 v[60:63], v[84:87], v[176:179], v[60:63]\n\t"
    "ds_read_b64_tr_b16 v[76:77], v104 offset:16384\n\t"
    "v_mfma_f32_16x16x32_bf16 v[64:67], v[84:87], v[180:183], v[64:67]\n\t"
    "ds_read_b64_tr_b16 v[78:79], v104 offset:16896\n\t"
    "s_add_u32 m0, s57, 0x6000\n\t"
    "s_mov_b32 s54, 0x36080\n\t"
    "buffer_load_dwordx4 v22, s[44:47], s54 offen  lds\n\t"
    "s_waitcnt lgkmcnt(7)\n\t"
    "v_mfma_f32_16x16x32_bf16 v[60:63], v[88:91], v[184:187], v[60:63]\n\t"
    "ds_read_b64_tr_b16 v[80:81], v104 offset:20480\n\t"
    "v_mfma_f32_16x16x32_bf16 v[64:67], v[88:91], v[188:191], v[64:67]\n\t"
    "ds_read_b64_tr_b16 v[82:83], v104 offset:20992\n\t"
    "s_waitcnt lgkmcnt(7)\n\t"
    "v_mfma_f32_16x16x32_bf16 v[60:63], v[92:95], v[192:195], v[60:63]\n\t"
    "s_waitcnt vmcnt(4) lgkmcnt(4)\n\t"
    "s_barrier\n\t"
    "ds_read_b64_tr_b16 v[84:85], v104 offset:24576\n\t"
    "v_mfma_f32_16x16x32_bf16 v[64:67], v[92:95], v[196:199], v[64:67]\n\t"
    "ds_read_b64_tr_b16 v[86:87], v104 offset:25088\n\t"
    "s_add_u32 m0, s57, 0x8000\n\t"
    "s_mov_b32 s54, 0x48080\n\t"
    "buffer_load_dwordx4 v22, s[44:47], s54 offen  lds\n\t"
    "ds_read_b128 v[240:243], v105 offset:32768\n\t"
    "ds_read_b128 v[244:247], v105 offset:34816\n\t"
    "v_mfma_f32_16x16x32_bf16 v[60:63], v[96:99], v[200:203], v[60:63]\n\t"
    "ds_read_b64_tr_b16 v[88:89], v104 offset:28672\n\t"
    "v_mfma_f32_16x16x32_bf16 v[64:67], v[96:99], v[204:207], v[64:67]\n\t"
    "ds_read_b64_tr_b16 v[90:91], v104 offset:29184\n\t"
    "ds_read_b128 v[248:251], v106 offset:32768\n\t"
    "ds_read_b128 v[252:255], v106 offset:34816\n\t"
    "s_waitcnt lgkmcnt(10)\n\t"
    "v_mfma_f32_16x16x32_bf16 v[60:63], v[76:79], v[208:211], v[60:63]\n\t"
    "ds_read_b64_tr_b16 v[92:93], v104 offset:32768\n\t"
    "v_mfma_f32_16x16x32_bf16 v[64:67], v[76:79], v[212:215], v[64:67]\n\t"
    "ds_read_b64_tr_b16 v[94:95], v104 offset:33280\n\t"
    "s_add_u32 m0, s57, 0xa000\n\t"
    "s_mov_b32 s54, 0x5a080\n\t"
    "buffer_load_dwordx4 v22, s[44:47], s54 offen  lds\n\t"
    "s_waitcnt lgkmcnt(10)\n\t"
    "v_mfma_f32_16x16x32_bf16 v[60:63], v[80:83], v[216:219], v[60:63]\n\t"
    "ds_read_b64_tr_b16 v[96:97], v104 offset:36864\n\t"
    "v_mfma_f32_16x16x32_bf16 v[64:67], v[80:83], v[220:223], v[64:67]\n\t"
    "ds_read_b64_tr_b16 v[98:99], v104 offset:37376\n\t"
    "s_waitcnt lgkmcnt(10)\n\t"
    "v_mfma_f32_16x16x32_bf16 v[60:63], v[84:87], v[224:227], v[60:63]\n\t"
    "s_waitcnt vmcnt(4) lgkmcnt(6)\n\t"
    "s_barrier\n\t"
    "ds_read_b64_tr_b16 v[76:77], v20 offset:0\n\t"
    "v_mfma_f32_16x16x32_bf16 v[64:67], v[84:87], v[228:231], v[64:67]\n\t"
    "ds_read_b64_tr_b16 v[78:79], v20 offset:512\n\t"
    "s_add_u32 m0, s57, 0xc000\n\t"
    "s_mov_b32 s54, 0x6c080\n\t"
    "buffer_load_dwordx4 v22, s[44:47], s54 offen  lds\n\t"
    "v_mfma_f32_16x16x32_bf16 v[60:63], v[88:91], v[232:235], v[60:63]\n\t"
    "ds_read_b64_tr_b16 v[80:81], v20 offset:4096\n\t"
    "v_mfma_f32_16x16x32_bf16 v[64:67], v[88:91], v[236:239], v[64:67]\n\t"
    "ds_read_b64_tr_b16 v[82:83], v20 offset:4608\n\t"
    "s_waitcnt lgkmcnt(6)\n\t"
    "v_mfma_f32_16x16x32_bf16 v[60:63], v[92:95], v[240:243], v[60:63]\n\t"
    "ds_read_b64_tr_b16 v[84:85], v20 offset:8192\n\t"
    "v_mfma_f32_16x16x32_bf16 v[64:67], v[92:95], v[244:247], v[64:67]\n\t"
    "ds_read_b64_tr_b16 v[86:87], v20 offset:8704\n\t"
    "s_add_u32 m0, s57, 0xe000\n\t"
    "s_mov_b32 s54, 0x7e080\n\t"
    "buffer_load_dwordx4 v22, s[44:47], s54 offen  lds\n\t"
    "s_waitcnt lgkmcnt(6)\n\t"
    "v_mfma_f32_16x16x32_bf16 v[68:71], v[96:99], v[248:251], v[60:63]\n\t"
    "ds_read_b64_tr_b16 v[88:89], v20 offset:12288\n\t"
    "v_mfma_f32_16x16x32_bf16 v[72:75], v[96:99], v[252:255], v[64:67]\n\t"
    "ds_read_b64_tr_b16 v[90:91], v20 offset:12800\n\t"
    "s_mov_b32 s55, 4\n\t"
    "s_movk_i32 s53, 0x80\n\t"
    "s_mov_b64 s[72:73], %[pxr]\n\t"
    "s_movk_i32 s74, 0x480\n\t"
    "s_mov_b64 s[76:77], %[pgw]\n\t"
    "s_movk_i32 s78, 0x480\n\t"
    "s_mov_b32 s79, 0x20000\n\t"
    "buffer_load_dwordx4 v[28:31], v107, s[72:75], 0 offen\n\t"
    "buffer_load_dwordx4 v[32:35], v107, s[72:75], 0 offen offset:1024\n\t"
    "buffer_load_dwordx4 v[36:39], v107, s[76:79], 0 offen\n\t"
    "buffer_load_dwordx4 v[40:43], v107, s[76:79], 0 offen offset:1024\n\t"
    "LC_%=:\n\t"
    "s_cmp_eq_u32 s55, 1\n\t"
    "s_cselect_b32 s62, 0, s62\n\t"
    "s_waitcnt lgkmcnt(6)\n\t"
    "v_mfma_f32_16x16x32_bf16 v[60:63], v[76:79], v[112:115], 0\n\t"
    "s_waitcnt vmcnt(4) lgkmcnt(4)\n\t"
    "s_barrier\n\t"
    "ds_read_b64_tr_b16 v[92:93], v20 offset:16384\n\t"
    "v_mfma_f32_16x16x32_bf16 v[64:67], v[76:79], v[116:119], 0\n\t"
    "ds_read_b64_tr_b16 v[94:95], v20 offset:16896\n\t"
    "s_sub_u32 s83, s83, 1\n\t"
    "s_cmp_eq_u32 s83, 0\n\t"
    "s_cbranch_scc0 LrC_%=\n\t"
    "global_load_dword v101, v[102:103], off \n\t"
    "LrC_%=:\n\t"
    "s_add_u32 m0, s57, 0x10000\n\t"
    "s_add_u32 s54, s53, 0x90000\n\t"
    "buffer_load_dwordx4 v22, s[44:47], s54 offen  lds\n\t"
    "v_mfma_f32_16x16x32_bf16 v[60:63], v[80:83], v[120:123], v[60:63]\n\t"
    "ds_read_b64_tr_b16 v[96:97], v20 offset:20480\n\t"
    "v_mfma_f32_16x16x32_bf16 v[64:67], v[80:83], v[124:127], v[64:67]\n\t"
    "ds_read_b64_tr_b16 v[98:99], v20 offset:20992\n\t"
    "s_waitcnt lgkmcnt(6)\n\t"
    "v_mfma_f32_16x16x32_bf16 v[60:63], v[84:87], v[128:131], v[60:63]\n\t"
    "ds_read_b64_tr_b16 v[76:77], v20 offset:24576\n\t"
    "v_cvt_pk_bf16_f32 v52, v68, v69\n\t"
    "v_mfma_f32_16x16x32_bf16 v[64:67], v[84:87], v[132:135], v[64:67]\n\t"
    "ds_read_b64_tr_b16 v[78:79], v20 offset:25088\n\t"
    "v_cvt_pk_bf16_f32 v53, v70, v71\n\t"
    "s_add_u32 m0, s57, 0x0\n\t"
    "s_add_u32 s54, s53, 0x80\n\t"
    "buffer_load_dwordx4 v22, s[44:47], s54 offen  lds\n\t"
    "s_waitcnt lgkmcnt(6)\n\t"
    "v_mfma_f32_16x16x32_bf16 v[60:63], v[88:91], v[136:139], v[60:63]\n\t"
    "ds_read_b64_tr_b16 v[80:81], v20 offset:28672\n\t"
    "v_cvt_pk_bf16_f32 v54, v72, v73\n\t"
    "v_mfma_f32_16x16x32_bf16 v[64:67], v[88:91], v[140:143], v[64:67]\n\t"
    "ds_read_b64_tr_b16 v[82:83], v20 offset:29184\n\t"
    "v_cvt_pk_bf16_f32 v55, v74, v75\n\t"
    "s_waitcnt lgkmcnt(6)\n\t"
    "v_mfma_f32_16x16x32_bf16 v[60:63], v[92:95], v[144:147], v[60:63]\n\t"
    "s_waitcnt vmcnt(4) lgkmcnt(4)\n\t"
    "s_barrier\n\t"
    "ds_read_b64_tr_b16 v[84:85], v104 offset:0\n\t"
    "v_mfma_f32_16x16x32_bf16 v[64:67], v[92:95], v[148:151], v[64:67]\n\t"
    "ds_read_b64_tr_b16 v[86:87], v104 offset:512\n\t"
    "s_add_u32 m0, s57, 0x2000\n\t"
    "s_add_u32 s54, s53, 0x12080\n\t"
    "buffer_load_dwordx4 v22, s[44:47], s54 offen  lds\n\t"
    "v_mfma_f32_16x16x32_bf16 v[60:63], v[96:99], v[152:155], v[60:63]\n\t"
    "ds_read_b64_tr_b16 v[88:89], v104 offset:4096\n\t"
    "v_mfma_f32_16x16x32_bf16 v[64:67], v[96:99], v[156:159], v[64:67]\n\t"
    "ds_read_b64_tr_b16 v[90:91], v104 offset:4608\n\t"
    "s_waitcnt lgkmcnt(6)\n\t"
    "v_mfma_f32_16x16x32_bf16 v[60:63], v[76:79], v[160:163], v[60:63]\n\t"
    "ds_read_b64_tr_b16 v[92:93], v104 offset:8192\n\t"
    "buffer_store_dwordx4 v[52:55], v23, s[48:51], s52 offen sc1\n\t"
    "s_add_u32 s52, s52, 0x40000\n\t"
    "v_mfma_f32_16x16x32_bf16 v[64:67], v[76:79], v[164:167], v[64:67]\n\t"
    "ds_read_b64_tr_b16 v[94:95], v104 offset:8704\n\t"
    "s_add_u32 m0, s57, 0x4000\n\t"
    "s_add_u32 s54, s53, 0x24080\n\t"
    "buffer_load_dwordx4 v22, s[44:47], s54 offen  lds\n\t"
    "s_waitcnt lgkmcnt(6)\n\t"
    "v_mfma_f32_16x16x32_bf16 v[60:63], v[80:83], v[168:171], v[60:63]\n\t"
    "ds_read_b64_tr_b16 v[96:97], v104 offset:12288\n\t"
    "v_mfma_f32_16x16x32_bf16 v[64:67], v[80:83], v[172:175], v[64:67]\n\t"
    "ds_read_b64_tr_b16 v[98:99], v104 offset:12800\n\t"
    "s_waitcnt lgkmcnt(6)\n\t"
    "v_mfma_f32_16x16x32_bf16 v[60:63], v[84:87], v[176:179], v[60:63]\n\t"
    "s_waitcnt vmcnt(5) lgkmcnt(4)\n\t"
    "s_barrier\n\t"
    "ds_read_b64_tr_b16 v[76:77], v104 offset:16384\n\t"
    "v_mfma_f32_16x16x32_bf16 v[64:67], v[84:87], v[180:183], v[64:67]\n\t"
    "ds_read_b64_tr_b16 v[78:79], v104 offset:16896\n\t"
    "s_add_u32 m0, s57, 0x6000\n\t"
    "s_add_u32 s54, s53, 0x36080\n\t"
    "buffer_load_dwordx4 v22, s[44:47], s54 offen  lds\n\t"
    "v_mfma_f32_16x16x32_bf16 v[60:63], v[88:91], v[184:187], v[60:63]\n\t"
    "ds_read_b64_tr_b16 v[80:81], v104 offset:20480\n\t"
    "v_mfma_f32_16x16x32_bf16 v[64:67], v[88:91], v[188:191], v[64:67]\n\t"
    "ds_read_b64_tr_b16 v[82:83], v104 offset:20992\n\t"
    "s_waitcnt lgkmcnt(6)\n\t"
    "v_mfma_f32_16x16x32_bf16 v[60:63], v[92:95], v[192:195], v[60:63]\n\t"
    "ds_read_b64_tr_b16 v[84:85], v104 offset:24576\n\t"
    "v_mfma_f32_16x16x32_bf16 v[64:67], v[92:95], v[196:199], v[64:67]\n\t"
    "ds_read_b64_tr_b16 v[86:87], v104 offset:25088\n\t"
    "s_add_u32 m0, s57, 0x8000\n\t"
    "s_add_u32 s54, s53, 0x48080\n\t"
    "buffer_load_dwordx4 v22, s[44:47], s54 offen  lds\n\t"
    "s_waitcnt lgkmcnt(6)\n\t"
    "v_mfma_f32_16x16x32_bf16 v[60:63], v[96:99], v[200:203], v[60:63]\n\t"
    "ds_read_b64_tr_b16 v[88:89], v104 offset:28672\n\t"
    "v_mfma_f32_16x16x32_bf16 v[64:67], v[96:99], v[204:207], v[64:67]\n\t"
    "ds_read_b64_tr_b16 v[90:91], v104 offset:29184\n\t"
    "s_waitcnt lgkmcnt(6)\n\t"
    "v_mfma_f32_16x16x32_bf16 v[60:63], v[76:79], v[208:211], v[60:63]\n\t"
    "s_waitcnt vmcnt(5) lgkmcnt(4)\n\t"
    "s_barrier\n\t"
    "ds_read_b64_tr_b16 v[92:93], v104 offset:32768\n\t"
    "v_mfma_f32_16x16x32_bf16 v[64:67], v[76:79], v[212:215], v[64:67]\n\t"
    "ds_read_b64_tr_b16 v[94:95], v104 offset:33280\n\t"
    "s_add_u32 m0, s57, 0xa000\n\t"
    "s_add_u32 s54, s53, 0x5a080\n\t"
    "buffer_load_dwordx4 v22, s[44:47], s54 offen  lds\n\t"
    "v_mfma_f32_16x16x32_bf16 v[60:63], v[80:83], v[216:219], v[60:63]\n\t"
    "ds_read_b64_tr_b16 v[96:97], v104 offset:36864\n\t"
    "v_mfma_f32_16x16x32_bf16 v[64:67], v[80:83], v[220:223], v[64:67]\n\t"
    "ds_read_b64_tr_b16 v[98:99], v104 offset:37376\n\t"
    "s_waitcnt lgkmcnt(6)\n\t"
    "v_mfma_f32_16x16x32_bf16 v[60:63], v[84:87], v[224:227], v[60:63]\n\t"
    "ds_read_b64_tr_b16 v[76:77], v20 offset:0\n\t"
    "v_mfma_f32_16x16x32_bf16 v[64:67], v[84:87], v[228:231], v[64:67]\n\t"
    "ds_read_b64_tr_b16 v[78:79], v20 offset:512\n\t"
    "s_add_u32 m0, s57, 0xc000\n\t"
    "s_add_u32 s54, s53, 0x6c080\n\t"
    "buffer_load_dwordx4 v22, s[44:47], s54 offen  lds\n\t"
    "s_waitcnt lgkmcnt(6)\n\t"
    "v_mfma_f32_16x16x32_bf16 v[60:63], v[88:91], v[232:235], v[60:63]\n\t"
    "ds_read_b64_tr_b16 v[80:81], v20 offset:4096\n\t"
    "v_mfma_f32_16x16x32_bf16 v[64:67], v[88:91], v[236:239], v[64:67]\n\t"
    "ds_read_b64_tr_b16 v[82:83], v20 offset:4608\n\t"
    "s_waitcnt lgkmcnt(6)\n\t"
    "v_mfma_f32_16x16x32_bf16 v[60:63], v[92:95], v[240:243], v[60:63]\n\t"
    "s_waitcnt vmcnt(4) lgkmcnt(4)\n\t"
    "s_barrier\n\t"
    "ds_read_b64_tr_b16 v[84:85], v20 offset:8192\n\t"
    "v_mfma_f32_16x16x32_bf16 v[64:67], v[92:95], v[244:247], v[64:67]\n\t"
    "ds_read_b64_tr_b16 v[86:87], v20 offset:8704\n\t"
    "s_add_u32 m0, s57, 0xe000\n\t"
    "s_add_u32 s54, s53, 0x7e080\n\t"
    "buffer_load_dwordx4 v22, s[44:47], s54 offen  lds\n\t"
    "v_mfma_f32_16x16x32_bf16 v[68:71], v[96:99], v[248:251], v[60:63]\n\t"
    "ds_read_b64_tr_b16 v[88:89], v20 offset:12288\n\t"
    "v_mfma_f32_16x16x32_bf16 v[72:75], v[96:99], v[252:255], v[64:67]\n\t"
    "ds_read_b64_tr_b16 v[90:91], v20 offset:12800\n\t"
    "s_waitcnt lgkmcnt(6)\n\t"
    "v_mfma_f32_16x16x32_bf16 v[60:63], v[76:79], v[112:115], 0\n\t"
    "ds_read_b64_tr_b16 v[92:93], v20 offset:16384\n\t"
    "v_mfma_f32_16x16x32_bf16 v[64:67], v[76:79], v[116:119], 0\n\t"
    "ds_read_b64_tr_b16 v[94:95], v20 offset:16896\n\t"
    "s_add_u32 m0, s57, 0x10000\n\t"
    "s_add_u32 s54, s53, 0x90080\n\t"
    "buffer_load_dwordx4 v22, s[44:47], s54 offen  lds\n\t"
    "s_waitcnt lgkmcnt(6)\n\t"
    "v_mfma_f32_16x16x32_bf16 v[60:63], v[80:83], v[120:123], v[60:63]\n\t"
    "ds_read_b64_tr_b16 v[96:97], v20 offset:20480\n\t"
    "v_mfma_f32_16x16x32_bf16 v[64:67], v[80:83], v[124:127], v[64:67]\n\t"
    "ds_read_b64_tr_b16 v[98:99], v20 offset:20992\n\t"
    "s_waitcnt lgkmcnt(6)\n\t"
    "v_mfma_f32_16x16x32_bf16 v[60:63], v[84:87], v[128:131], v[60:63]\n\t"
    "s_waitcnt vmcnt(4) lgkmcnt(4)\n\t"
    "s_barrier\n\t"
    "ds_read_b64_tr_b16 v[76:77], v20 offset:24576\n\t"
    "v_cvt_pk_bf16_f32 v52, v68, v69\n\t"
    "v_mfma_f32_16x16x32_bf16 v[64:67], v[84:87], v[132:135], v[64:67]\n\t"
    "ds_read_b64_tr_b16 v[78:79], v20 offset:25088\n\t"
    "v_cvt_pk_bf16_f32 v53, v70, v71\n\t"
    "s_add_u32 m0, s57, 0x0\n\t"
    "s_add_u32 s54, s53, 0x100\n\t"
    "buffer_load_dwordx4 v22, s[60:63], s54 offen  lds\n\t"
    "v_mfma_f32_16x16x32_bf16 v[60:63], v[88:91], v[136:139], v[60:63]\n\t"
    "ds_read_b64_tr_b16 v[80:81], v20 offset:28672\n\t"
    "v_cvt_pk_bf16_f32 v54, v72, v73\n\t"
    "v_mfma_f32_16x16x32_bf16 v[64:67], v[88:91], v[140:143], v[64:67]\n\t"
    "ds_read_b64_tr_b16 v[82:83], v20 offset:29184\n\t"
    "v_cvt_pk_bf16_f32 v55, v74, v75\n\t"
    "s_waitcnt lgkmcnt(6)\n\t"
    "v_mfma_f32_16x16x32_bf16 v[60:63], v[92:95], v[144:147], v[60:63]\n\t"
    "ds_read_b64_tr_b16 v[84:85], v104 offset:0\n\t"
    "v_mfma_f32_16x16x32_bf16 v[64:67], v[92:95], v[148:151], v[64:67]\n\t"
    "ds_read_b64_tr_b16 v[86:87], v104 offset:512\n\t"
    "s_add_u32 m0, s57, 0x2000\n\t"
    "s_add_u32 s54, s53, 0x12100\n\t"
    "buffer_load_dwordx4 v22, s[60:63], s54 offen  lds\n\t"
    "s_waitcnt lgkmcnt(6)\n\t"
    "v_mfma_f32_16x16x32_bf16 v[60:63], v[96:99], v[152:155], v[60:63]\n\t"
    "ds_read_b64_tr_b16 v[88:89], v104 offset:4096\n\t"
    "v_mfma_f32_16x16x32_bf16 v[64:67], v[96:99], v[156:159], v[64:67]\n\t"
    "ds_read_b64_tr_b16 v[90:91], v104 offset:4608\n\t"
    "s_waitcnt lgkmcnt(6)\n\t"
    "v_mfma_f32_16x16x32_bf16 v[60:63], v[76:79], v[160:163], v[60:63]\n\t"
    "s_waitcnt vmcnt(4) lgkmcnt(4)\n\t"
    "s_barrier\n\t"
    "ds_read_b64_tr_b16 v[92:93], v104 offset:8192\n\t"
    "buffer_store_dwordx4 v[52:55], v23, s[48:51], s52 offen sc1\n\t"
    "s_add_u32 s52, s52, 0x40000\n\t"
    "v_mfma_f32_16x16x32_bf16 v[64:67], v[76:79], v[164:167], v[64:67]\n\t"
    "ds_read_b64_tr_b16 v[94:95], v104 offset:8704\n\t"
    "s_add_u32 m0, s57, 0x4000\n\t"
    "s_add_u32 s54, s53, 0x24100\n\t"
    "buffer_load_dwordx4 v22, s[60:63], s54 offen  lds\n\t"
    "v_mfma_f32_16x16x32_bf16 v[60:63], v[80:83], v[168:171], v[60:63]\n\t"
    "ds_read_b64_tr_b16 v[96:97], v104 offset:12288\n\t"
    "v_mfma_f32_16x16x32_bf16 v[64:67], v[80:83], v[172:175], v[64:67]\n\t"
    "ds_read_b64_tr_b16 v[98:99], v104 offset:12800\n\t"
    "s_waitcnt lgkmcnt(6)\n\t"
    "v_mfma_f32_16x16x32_bf16 v[60:63], v[84:87], v[176:179], v[60:63]\n\t"
    "ds_read_b64_tr_b16 v[76:77], v104 offset:16384\n\t"
    "v_mfma_f32_16x16x32_bf16 v[64:67], v[84:87], v[180:183], v[64:67]\n\t"
    "ds_read_b64_tr_b16 v[78:79], v104 offset:16896\n\t"
    "s_add_u32 m0, s57, 0x6000\n\t"
    "s_add_u32 s54, s53, 0x36100\n\t"
    "buffer_load_dwordx4 v22, s[60:63], s54 offen  lds\n\t"
    "s_waitcnt lgkmcnt(6)\n\t"
    "v_mfma_f32_16x16x32_bf16 v[60:63], v[88:91], v[184:187], v[60:63]\n\t"
    "ds_read_b64_tr_b16 v[80:81], v104 offset:20480\n\t"
    "v_mfma_f32_16x16x32_bf16 v[64:67], v[88:91], v[188:191], v[64:67]\n\t"
    "ds_read_b64_tr_b16 v[82:83], v104 offset:20992\n\t"
    "s_waitcnt lgkmcnt(6)\n\t"
    "v_mfma_f32_16x16x32_bf16 v[60:63], v[92:95], v[192:195], v[60:63]\n\t"
    "s_waitcnt vmcnt(5) lgkmcnt(4)\n\t"
    "s_barrier\n\t"
    "ds_read_b64_tr_b16 v[84:85], v104 offset:24576\n\t"
    "v_mfma_f32_16x16x32_bf16 v[64:67], v[92:95], v[196:199], v[64:67]\n\t"
    "ds_read_b64_tr_b16 v[86:87], v104 offset:25088\n\t"
    "s_add_u32 m0, s57, 0x8000\n\t"
    "s_add_u32 s54, s53, 0x48100\n\t"
    "buffer_load_dwordx4 v22, s[60:63], s54 offen  lds\n\t"
    "v_mfma_f32_16x16x32_bf16 v[60:63], v[96:99], v[200:203], v[60:63]\n\t"
    "ds_read_b64_tr_b16 v[88:89], v104 offset:28672\n\t"
    "v_mfma_f32_16x16x32_bf16 v[64:67], v[96:99], v[204:207], v[64:67]\n\t"
    "ds_read_b64_tr_b16 v[90:91], v104 offset:29184\n\t"
    "s_waitcnt lgkmcnt(6)\n\t"
    "v_mfma_f32_16x16x32_bf16 v[60:63], v[76:79], v[208:211], v[60:63]\n\t"
    "ds_read_b64_tr_b16 v[92:93], v104 offset:32768\n\t"
    "v_mfma_f32_16x16x32_bf16 v[64:67], v[76:79], v[212:215], v[64:67]\n\t"
    "ds_read_b64_tr_b16 v[94:95], v104 offset:33280\n\t"
    "s_add_u32 m0, s57, 0xa000\n\t"
    "s_add_u32 s54, s53, 0x5a100\n\t"
    "buffer_load_dwordx4 v22, s[60:63], s54 offen  lds\n\t"
    "s_waitcnt lgkmcnt(6)\n\t"
    "v_mfma_f32_16x16x32_bf16 v[60:63], v[80:83], v[216:219], v[60:63]\n\t"
    "ds_read_b64_tr_b16 v[96:97], v104 offset:36864\n\t"
    "v_mfma_f32_16x16x32_bf16 v[64:67], v[80:83], v[220:223], v[64:67]\n\t"
    "ds_read_b64_tr_b16 v[98:99], v104 offset:37376\n\t"
    "s_waitcnt lgkmcnt(6)\n\t"
    "v_mfma_f32_16x16x32_bf16 v[60:63], v[84:87], v[224:227], v[60:63]\n\t"
    "s_waitcnt vmcnt(5) lgkmcnt(4)\n\t"
    "s_barrier\n\t"
    "ds_read_b64_tr_b16 v[76:77], v20 offset:0\n\t"
    "v_mfma_f32_16x16x32_bf16 v[64:67], v[84:87], v[228:231], v[64:67]\n\t"
    "ds_read_b64_tr_b16 v[78:79], v20 offset:512\n\t"
    "s_add_u32 m0, s57, 0xc000\n\t"
    "s_add_u32 s54, s53, 0x6c100\n\t"
    "buffer_load_dwordx4 v22, s[60:63], s54 offen  lds\n\t"
    "v_mfma_f32_16x16x32_bf16 v[60:63], v[88:91], v[232:235], v[60:63]\n\t"
    "ds_read_b64_tr_b16 v[80:81], v20 offset:4096\n\t"
    "v_mfma_f32_16x16x32_bf16 v[64:67], v[88:91], v[236:239], v[64:67]\n\t"
    "ds_read_b64_tr_b16 v[82:83], v20 offset:4608\n\t"
    "s_waitcnt lgkmcnt(6)\n\t"
    "v_mfma_f32_16x16x32_bf16 v[60:63], v[92:95], v[240:243], v[60:63]\n\t"
    "ds_read_b64_tr_b16 v[84:85], v20 offset:8192\n\t"
    "v_mfma_f32_16x16x32_bf16 v[64:67], v[92:95], v[244:247], v[64:67]\n\t"
    "ds_read_b64_tr_b16 v[86:87], v20 offset:8704\n\t"
    "s_add_u32 m0, s57, 0xe000\n\t"
    "s_add_u32 s54, s53, 0x7e100\n\t"
    "buffer_load_dwordx4 v22, s[60:63], s54 offen  lds\n\t"
    "s_waitcnt lgkmcnt(6)\n\t"
    "v_mfma_f32_16x16x32_bf16 v[68:71], v[96:99], v[248:251], v[60:63]\n\t"
    "ds_read_b64_tr_b16 v[88:89], v20 offset:12288\n\t"
    "v_mfma_f32_16x16x32_bf16 v[72:75], v[96:99], v[252:255], v[64:67]\n\t"
    "ds_read_b64_tr_b16 v[90:91], v20 offset:12800\n\t"
    "s_add_u32 s53, s53, 0x100\n\t"
    "s_sub_u32 s55, s55, 1\n\t"
    "s_cmp_lg_u32 s55, 0\n\t"
    "s_cbranch_scc1 LC_%=\n\t"
    "s_nop 7\n\t"
    "v_cvt_pk_bf16_f32 v52, v68, v69\n\t"
    "v_cvt_pk_bf16_f32 v53, v70, v71\n\t"
    "v_cvt_pk_bf16_f32 v54, v72, v73\n\t"
    "v_cvt_pk_bf16_f32 v55, v74, v75\n\t"
    "buffer_store_dwordx4 v[52:55], v23, s[48:51], s52 offen sc1\n\t"
    "s_waitcnt vmcnt(63)\n\t"
    "v_mov_b32_e32 v44, 0\n\t"
    "s_mov_b64 s[72:73], %[pg]\n\t"
    "s_movk_i32 s74, 0x2000\n\t"
    "v_dot2_f32_bf16 v44, v28, v36, v44\n\t"
    "v_dot2_f32_bf16 v44, v29, v37, v44\n\t"
    "v_dot2_f32_bf16 v44, v30, v38, v44\n\t"
    "v_dot2_f32_bf16 v44, v31, v39, v44\n\t"
    "v_dot2_f32_bf16 v44, v32, v40, v44\n\t"
    "v_dot2_f32_bf16 v44, v33, v41, v44\n\t"
    "v_dot2_f32_bf16 v44, v34, v42, v44\n\t"
    "v_dot2_f32_bf16 v44, v35, v43, v44\n\t"
    "s_nop 1\n\t"
    "s_nop 1\n\t"
    "v_add_f32_dpp v44, v44, v44 row_shr:1 row_mask:0xf bank_mask:0xf bound_ctrl:0\n\t"
    "s_nop 1\n\t"
    "v_add_f32_dpp v44, v44, v44 row_shr:2 row_mask:0xf bank_mask:0xf bound_ctrl:0\n\t"
    "s_nop 1\n\t"
    "v_add_f32_dpp v44, v44, v44 row_shr:4 row_mask:0xf bank_mask:0xf bound_ctrl:0\n\t"
    "s_nop 1\n\t"
    "v_add_f32_dpp v44, v44, v44 row_shr:8 row_mask:0xf bank_mask:0xf bound_ctrl:0\n\t"
    "s_nop 1\n\t"
    "v_add_f32_dpp v44, v44, v44 row_bcast:15 row_mask:0xa bank_mask:0xf\n\t"
    "s_nop 1\n\t"
    "v_add_f32_dpp v44, v44, v44 row_bcast:31 row_mask:0xc bank_mask:0xf\n\t"
    "v_mul_f32_e64 v44, -v44, s56\n\t"
    "v_exp_f32_e32 v44, v44\n\t"
    "s_nop 0\n\t"
    "v_add_f32_e32 v44, 1.0, v44\n\t"
    "v_rcp_f32_e32 v44, v44\n\t"
    "s_nop 0\n\t"
    "buffer_store_dword v44, v108, s[72:75], 0 offen sc1\n\t"
    "s_waitcnt lgkmcnt(0)\n\t"
    :
    : [pw1]"s"(w1),[pw2]"s"(w2),[pp]"s"(P),[ldsw]"s"(lds+w*1024),[pg]"s"(G),[pxt]"s"(XT),[when]"s"(when),[sg4]"s"(grow*4),[pxr]"s"(x+grow*D),[pgw]"s"(gw)
    : "memory","scc","vcc",K1_CLOB);
  if(has){
    unsigned ln=threadIdx.x&63; asm volatile("":"+v"(ln));
    const unsigned m=(kh?127:63)-ln, sh=kh?3:(m>=32?2:(m>=16?1:0));
    const unsigned* Fl=(const unsigned*)(Fc+128*sh+((16*m)>>sh));
    unsigned v[9],o[8];
#pragma unroll
    for(int j=0;j<9;j++) v[j]=Fl[j];
    if(kh) expand16<3>(v,o); else if(sh==2) expand16<2>(v,o); else if(sh==1) expand16<1>(v,o); else expand16<0>(v,o);
    char* kp=KR+dch*KRS+32*(127-m);
    *(u32x4*)kp=u32x4{o[0],o[1],o[2],o[3]}; *(u32x4*)(kp+16)=u32x4{o[4],o[5],o[6],o[7]};
    if(m==0) *(u32x4*)(kp+32)=u32x4{0,0,0,0};
  }
}

constexpr int KCS=4160, KCH=4*KCS;
constexpr int O_U=64, UCH=4608, O_K=O_U+4*UCH+64, O_Y=O_K+4*KCH, K2_LDS=O_Y+4*8192;
static_assert(O_K%16==0 && K2_LDS<=163840,"lds map");
#define GLB __attribute__((address_space(1)))
constexpr int K2_CODE_BYTES=5888;
DI float fadd1(float a,float b){ float r; asm("v_add_f32_e32 %0, %1, %2":"=v"(r):"v"(a),"v"(b)); return r; }
DI void k2_sum(const u32x4 (&pv)[8],f32x2 gv,float (&pre)[8]){
  float a[8]={blo(pv[0][0]),bhi(pv[0][0]),blo(pv[0][1]),bhi(pv[0][1]),blo(pv[0][2]),bhi(pv[0][2]),blo(pv[0][3]),bhi(pv[0][3])};
#pragma unroll
  for(int s=1;s<8;s++)
#pragma unroll
    for(int j=0;j<4;j++){ a[2*j]=fadd1(a[2*j],blo(pv[s][j])); a[2*j+1]=fadd1(a[2*j+1],bhi(pv[s][j])); }
  const float s0=(1.f-gv[0])*0.69314718056f, s1=(1.f-gv[1])*0.69314718056f;
#pragma unroll
  for(int c=0;c<4;c++){ pre[c]=s0*a[c]; pre[4+c]=s1*a[4+c]; }
  asm volatile("":"+v"(pre[0]),"+v"(pre[1]),"+v"(pre[2]),"+v"(pre[3]),"+v"(pre[4]),"+v"(pre[5]),"+v"(pre[6]),"+v"(pre[7]));
}
DI void k2_fin(const f32x2 (&y)[4],f32x2 gv,const float (&pre)[8],char* op){
  float o[8];
#pragma unroll
  for(int c=0;c<4;c++){ o[c]=fmaf(gv[0],y[c][0],pre[c]); o[4+c]=fmaf(gv[1],y[c][1],pre[4+c]); }
  *(u32x2*)op=u32x2{cvtpk(o[0],o[1]),cvtpk(o[2],o[3])}; *(u32x2*)(op+2*D)=u32x2{cvtpk(o[4],o[5]),cvtpk(o[6],o[7])};
}
extern "C" __global__ __launch_bounds__(512,2) void k2_ssm_mix(const u16* __restrict__ XT,const char* __restrict__ KR,const u16* __restrict__ P,const float* __restrict__ G,u16* __restrict__ out
){
  __shared__ __attribute__((aligned(1024))) char smem[K2_LDS];
  const unsigned b=blockIdx.x, dg=(b&7)*18+(b>>3), lane=threadIdx.x&63, w=rfl(threadIdx.x>>6), lds=(unsigned)(size_t)smem;
  if(threadIdx.x<4) *(unsigned*)(smem+4*threadIdx.x)=0;
  BAR();
  if(w<4){
    const unsigned d=4*dg+w, li=lane&15, g=lane>>4, qc=3-(li&3);
    asm volatile(
    "s_mov_b64 s[40:41], %[px]\n\t"
    "s_mov_b32 s42, 0x240000\n\t"
    "s_mov_b32 s43, 0x20000\n\t"
    "s_mov_b64 s[44:45], %[pk]\n\t"
    "s_mov_b32 s46, 0x252000\n\t"
    "s_mov_b32 s47, 0x20000\n\t"
    "s_mov_b32 m0, %[lu]\n\t"
    "s_nop 0\n\t"
    "buffer_load_dwordx4 %[vo], s[40:43], %[su] offen offset:0 lds\n\t"
    "buffer_load_dwordx4 %[vo], s[40:43], %[su] offen offset:1024 lds\n\t"
    "s_add_u32 m0, %[lk], 0\n\t"
    "s_add_u32 s48, %[sk], 0\n\t"
    "buffer_load_dwordx4 %[vo], s[44:47], s48 offen offset:3072 lds\n\t"
    "buffer_load_dwordx4 %[vo], s[44:47], s48 offen offset:2048 lds\n\t"
    "s_add_u32 m0, %[lk], 4160\n\t"
    "s_add_u32 s48, %[sk], 2\n\t"
    "buffer_load_dwordx4 %[vo], s[44:47], s48 offen offset:3072 lds\n\t"
    "buffer_load_dwordx4 %[vo], s[44:47], s48 offen offset:2048 lds\n\t"
    "s_add_u32 m0, %[lk], 8320\n\t"
    "s_add_u32 s48, %[sk], 4\n\t"
    "buffer_load_dwordx4 %[vo], s[44:47], s48 offen offset:3072 lds\n\t"
    "buffer_load_dwordx4 %[vo], s[44:47], s48 offen offset:2048 lds\n\t"
    "s_add_u32 m0, %[lk], 12480\n\t"
    "s_add_u32 s48, %[sk], 6\n\t"
    "buffer_load_dwordx4 %[vo], s[44:47], s48 offen offset:3072 lds\n\t"
    "buffer_load_dwordx4 %[vo], s[44:47], s48 offen offset:2048 lds\n\t"
    "s_mov_b32 m0, %[lu]\n\t"
    "s_nop 0\n\t"
    "buffer_load_dwordx4 %[vo], s[40:43], %[su] offen offset:2048 lds\n\t"
    "buffer_load_dwordx4 %[vo], s[40:43], %[su] offen offset:3072 lds\n\t"
    "s_add_u32 m0, %[lk], 0\n\t"
    "s_add_u32 s48, %[sk], 0\n\t"
    "buffer_load_dwordx4 %[vo], s[44:47], s48 offen offset:1024 lds\n\t"
    "buffer_load_dwordx4 %[vo], s[44:47], s48 offen offset:0 lds\n\t"
    "s_add_u32 m0, %[lk], 4160\n\t"
    "s_add_u32 s48, %[sk], 2\n\t"
    "buffer_load_dwordx4 %[vo], s[44:47], s48 offen offset:1024 lds\n\t"
    "buffer_load_dwordx4 %[vo], s[44:47], s48 offen offset:0 lds\n\t"
    "s_add_u32 m0, %[lk], 8320\n\t"
    "s_add_u32 s48, %[sk], 4\n\t"
    "buffer_load_dwordx4 %[vo], s[44:47], s48 offen offset:1024 lds\n\t"
    "buffer_load_dwordx4 %[vo], s[44:47], s48 offen offset:0 lds\n\t"
    "s_add_u32 m0, %[lk], 12480\n\t"
    "s_add_u32 s48, %[sk], 6\n\t"
    "buffer_load_dwordx4 %[vo], s[44:47], s48 offen offset:1024 lds\n\t"
    "buffer_load_dwordx4 %[vo], s[44:47], s48 offen offset:0 lds\n\t"
      :: [px]"s"(XT),[pk]"s"(KR),[lu]"s"(lds+O_U+w*UCH+512),[lk]"s"(lds+O_K+w*KCH),[su]"s"(d*4096),[sk]"s"(d*KRS),[vo]"v"(lane*16)
      : "memory","scc","s40","s41","s42","s43","s44","s45","s46","s47","s48");
    if(lane<32) *(u32x4*)(smem+O_U+w*UCH+16*lane)=u32x4{0,0,0,0};
    else if(lane<48) *(u32x4*)(smem+O_K+w*KCH+((lane>>2)&3)*KCS+4096+16*(lane&3))=u32x4{0,0,0,0};
    unsigned va=lds+O_K+w*KCH+qc*KCS+2*(2047-16*113-li+8*g-qc);
    unsigned vb=lds+O_U+w*UCH+2*(256-16+16*li+8*g);
    SB(); asm volatile("s_waitcnt vmcnt(10) lgkmcnt(0)":::"memory"); SB();
    TS(0);
    asm volatile(
      "ds_read_b64 v[96:97], %[va] offset:3584\n\t"
      "ds_read_b64 v[98:99], %[va] offset:3592\n\t"
      "ds_read_b128 v[140:143], %[vb] offset:1536\n\t"
      "ds_read_b128 v[136:139], %[vb] offset:1024\n\t"
      "ds_read_b128 v[132:135], %[vb] offset:512\n\t"
      "ds_read_b128 v[128:131], %[vb] offset:0\n\t"
      "ds_read_b64 v[100:101], %[va] offset:3072\n\t"
      "ds_read_b64 v[102:103], %[va] offset:3080\n\t"
      "ds_read_b64 v[106:107], %[va] offset:2568\n\t"
      "ds_read_b64 v[110:111], %[va] offset:2056\n\t"
      "v_subrev_u32_e32 %[va], 64, %[va]\n\t"
      "v_subrev_u32_e32 %[vb], 64, %[vb]\n\t"
      "s_mov_b32 s40, 8\n\t"
      "v_mov_b32_e32 v64, 0\n\t"
      "v_mov_b32_e32 v65, 0\n\t"
      "v_mov_b32_e32 v66, 0\n\t"
      "v_mov_b32_e32 v67, 0\n\t"
      "v_mov_b32_e32 v68, 0\n\t"
      "v_mov_b32_e32 v69, 0\n\t"
      "v_mov_b32_e32 v70, 0\n\t"
      "v_mov_b32_e32 v71, 0\n\t"
      "v_mov_b32_e32 v72, 0\n\t"
      "v_mov_b32_e32 v73, 0\n\t"
      "v_mov_b32_e32 v74, 0\n\t"
      "v_mov_b32_e32 v75, 0\n\t"
      "v_mov_b32_e32 v76, 0\n\t"
      "v_mov_b32_e32 v77, 0\n\t"
      "v_mov_b32_e32 v78, 0\n\t"
      "v_mov_b32_e32 v79, 0\n\t"
      "s_nop 1\n\t"
      "Lc0_%=:\n\t"
      "s_waitcnt lgkmcnt(7)\n\t"
      "v_mfma_f32_16x16x32_bf16 v[76:79], v[96:99], v[140:143], v[76:79]\n\t"
      "ds_read_b128 v[140:143], %[vb] offset:1536\n\t"
      "s_waitcnt lgkmcnt(7)\n\t"
      "v_mfma_f32_16x16x32_bf16 v[72:75], v[96:99], v[136:139], v[72:75]\n\t"
      "ds_read_b64 v[104:105], %[va] offset:2624\n\t"
      "s_waitcnt lgkmcnt(6)\n\t"
      "v_mfma_f32_16x16x32_bf16 v[68:71], v[96:99], v[132:135], v[68:71]\n\t"
      "ds_read_b64 v[108:109], %[va] offset:2112\n\t"
      "s_waitcnt lgkmcnt(4)\n\t"
      "v_mfma_f32_16x16x32_bf16 v[64:67], v[96:99], v[128:131], v[64:67]\n\t"
      "ds_read_b64 v[96:97], %[va] offset:3584\n\t"
      "s_waitcnt lgkmcnt(6)\n\t"
      "v_mfma_f32_16x16x32_bf16 v[76:79], v[100:103], v[136:139], v[76:79]\n\t"
      "ds_read_b64 v[98:99], %[va] offset:3592\n\t"
      "v_mfma_f32_16x16x32_bf16 v[72:75], v[100:103], v[132:135], v[72:75]\n\t"
      "ds_read_b128 v[136:139], %[vb] offset:1024\n\t"
      "v_mfma_f32_16x16x32_bf16 v[68:71], v[100:103], v[128:131], v[68:71]\n\t"
      "ds_read_b64 v[100:101], %[va] offset:3072\n\t"
      "s_waitcnt lgkmcnt(5)\n\t"
      "v_mfma_f32_16x16x32_bf16 v[76:79], v[104:107], v[132:135], v[76:79]\n\t"
      "ds_read_b128 v[132:135], %[vb] offset:512\n\t"
      "v_mfma_f32_16x16x32_bf16 v[72:75], v[104:107], v[128:131], v[72:75]\n\t"
      "ds_read_b64 v[102:103], %[va] offset:3080\n\t"
      "ds_read_b64 v[106:107], %[va] offset:2568\n\t"
      "s_waitcnt lgkmcnt(7)\n\t"
      "v_mfma_f32_16x16x32_bf16 v[76:79], v[108:111], v[128:131], v[76:79]\n\t"
      "ds_read_b128 v[128:131], %[vb] offset:0\n\t"
      "ds_read_b64 v[110:111], %[va] offset:2056\n\t"
      "v_subrev_u32_e32 %[va], 64, %[va]\n\t"
      "v_subrev_u32_e32 %[vb], 64, %[vb]\n\t"
      "s_sub_u32 s40, s40, 1\n\t"
      "s_cmp_lg_u32 s40, 0\n\t"
      "s_cbranch_scc1 Lc0_%=\n\t"
      "v_add_u32_e32 %[va], 0x240, %[va]\n\t"
      "v_add_u32_e32 %[vb], 0x240, %[vb]\n\t"
      "s_nop 2\n\t"
      "ds_write_b128 %[vy], v[64:67] offset:0\n\t"
      "ds_write_b128 %[vy], v[68:71] offset:1024\n\t"
      "ds_write_b128 %[vy], v[72:75] offset:2048\n\t"
      "ds_write_b128 %[vy], v[76:79] offset:3072\n\t"
      "s_waitcnt vmcnt(0)\n\t"
      "ds_read_b64 v[96:97], %[va] offset:3584\n\t"
      "ds_read_b64 v[98:99], %[va] offset:3592\n\t"
      "ds_read_b128 v[156:159], %[vb] offset:3584\n\t"
      "ds_read_b128 v[152:155], %[vb] offset:3072\n\t"
      "ds_read_b128 v[148:151], %[vb] offset:2560\n\t"
      "ds_read_b128 v[144:147], %[vb] offset:2048\n\t"
      "ds_read_b64 v[100:101], %[va] offset:3072\n\t"
      "ds_read_b64 v[102:103], %[va] offset:3080\n\t"
      "ds_read_b128 v[140:143], %[vb] offset:1536\n\t"
      "ds_read_b64 v[104:105], %[va] offset:2560\n\t"
      "ds_read_b64 v[106:107], %[va] offset:2568\n\t"
      "ds_read_b128 v[136:139], %[vb] offset:1024\n\t"
      "ds_read_b64 v[108:109], %[va] offset:2048\n\t"
      "ds_read_b64 v[110:111], %[va] offset:2056\n\t"
      "ds_read_b128 v[132:135], %[vb] offset:512\n\t"
      "ds_read_b64 v[112:113], %[va] offset:1536\n\t"
      "ds_read_b64 v[114:115], %[va] offset:1544\n\t"
      "ds_read_b128 v[128:131], %[vb] offset:0\n\t"
      "ds_read_b64 v[116:117], %[va] offset:1024\n\t"
      "ds_read_b64 v[118:119], %[va] offset:1032\n\t"
      "v_subrev_u32_e32 %[va], 64, %[va]\n\t"
      "v_subrev_u32_e32 %[vb], 64, %[vb]\n\t"
      "s_mov_b32 s40, 8\n\t"
      "v_mov_b32_e32 v80, 0\n\t"
      "v_mov_b32_e32 v81, 0\n\t"
      "v_mov_b32_e32 v82, 0\n\t"
      "v_mov_b32_e32 v83, 0\n\t"
      "v_mov_b32_e32 v84, 0\n\t"
      "v_mov_b32_e32 v85, 0\n\t"
      "v_mov_b32_e32 v86, 0\n\t"
      "v_mov_b32_e32 v87, 0\n\t"
      "v_mov_b32_e32 v88, 0\n\t"
      "v_mov_b32_e32 v89, 0\n\t"
      "v_mov_b32_e32 v90, 0\n\t"
      "v_mov_b32_e32 v91, 0\n\t"
      "v_mov_b32_e32 v92, 0\n\t"
      "v_mov_b32_e32 v93, 0\n\t"
      "v_mov_b32_e32 v94, 0\n\t"
      "v_mov_b32_e32 v95, 0\n\t"
      "s_waitcnt lgkmcnt(15)\n\t"
      "ds_write_b32 %[vf], %[one]\n\t"
      "s_nop 1\n\t"
      "Lc1_%=:\n\t"
      "s_waitcnt lgkmcnt(15)\n\t"
      "v_mfma_f32_16x16x32_bf16 v[92:95], v[96:99], v[156:159], v[92:95]\n\t"
      "ds_read_b128 v[156:159], %[vb] offset:3584\n\t"
      "v_mfma_f32_16x16x32_bf16 v[88:91], v[96:99], v[152:155], v[88:91]\n\t"
      "ds_read_b64 v[120:121], %[va] offset:576\n\t"
      "v_mfma_f32_16x16x32_bf16 v[84:87], v[96:99], v[148:151], v[84:87]\n\t"
      "ds_read_b64 v[122:123], %[va] offset:584\n\t"
      "s_waitcnt lgkmcnt(14)\n\t"
      "v_mfma_f32_16x16x32_bf16 v[80:83], v[96:99], v[144:147], v[80:83]\n\t"
      "ds_read_b64 v[96:97], %[va] offset:3584\n\t"
      "s_waitcnt lgkmcnt(15)\n\t"
      "v_mfma_f32_16x16x32_bf16 v[92:95], v[100:103], v[152:155], v[92:95]\n\t"
      "ds_read_b64 v[98:99], %[va] offset:3592\n\t"
      "v_mfma_f32_16x16x32_bf16 v[88:91], v[100:103], v[148:151], v[88:91]\n\t"
      "ds_read_b128 v[152:155], %[vb] offset:3072\n\t"
      "v_mfma_f32_16x16x32_bf16 v[84:87], v[100:103], v[144:147], v[84:87]\n\t"
      "ds_read_b64 v[124:125], %[va] offset:64\n\t"
      "s_waitcnt lgkmcnt(15)\n\t"
      "v_mfma_f32_16x16x32_bf16 v[80:83], v[100:103], v[140:143], v[80:83]\n\t"
      "ds_read_b64 v[100:101], %[va] offset:3072\n\t"
      "s_waitcnt lgkmcnt(15)\n\t"
      "v_mfma_f32_16x16x32_bf16 v[92:95], v[104:107], v[148:151], v[92:95]\n\t"
      "ds_read_b128 v[148:151], %[vb] offset:2560\n\t"
      "v_mfma_f32_16x16x32_bf16 v[88:91], v[104:107], v[144:147], v[88:91]\n\t"
      "ds_read_b64 v[102:103], %[va] offset:3080\n\t"
      "v_mfma_f32_16x16x32_bf16 v[84:87], v[104:107], v[140:143], v[84:87]\n\t"
      "ds_read_b64 v[126:127], %[va] offset:72\n\t"
      "s_waitcnt lgkmcnt(15)\n\t"
      "v_mfma_f32_16x16x32_bf16 v[80:83], v[104:107], v[136:139], v[80:83]\n\t"
      "ds_read_b64 v[104:105], %[va] offset:2560\n\t"
      "v_mfma_f32_16x16x32_bf16 v[92:95], v[108:111], v[144:147], v[92:95]\n\t"
      "ds_read_b128 v[144:147], %[vb] offset:2048\n\t"
      "v_mfma_f32_16x16x32_bf16 v[88:91], v[108:111], v[140:143], v[88:91]\n\t"
      "ds_read_b64 v[106:107], %[va] offset:2568\n\t"
      "v_mfma_f32_16x16x32_bf16 v[84:87], v[108:111], v[136:139], v[84:87]\n\t"
      "s_waitcnt lgkmcnt(15)\n\t"
      "v_mfma_f32_16x16x32_bf16 v[80:83], v[108:111], v[132:135], v[80:83]\n\t"
      "ds_read_b64 v[108:109], %[va] offset:2048\n\t"
      "v_mfma_f32_16x16x32_bf16 v[92:95], v[112:115], v[140:143], v[92:95]\n\t"
      "ds_read_b128 v[140:143], %[vb] offset:1536\n\t"
      "v_mfma_f32_16x16x32_bf16 v[88:91], v[112:115], v[136:139], v[88:91]\n\t"
      "ds_read_b64 v[110:111], %[va] offset:2056\n\t"
      "v_mfma_f32_16x16x32_bf16 v[84:87], v[112:115], v[132:135], v[84:87]\n\t"
      "s_waitcnt lgkmcnt(15)\n\t"
      "v_mfma_f32_16x16x32_bf16 v[80:83], v[112:115], v[128:131], v[80:83]\n\t"
      "ds_read_b64 v[112:113], %[va] offset:1536\n\t"
      "v_mfma_f32_16x16x32_bf16 v[92:95], v[116:119], v[136:139], v[92:95]\n\t"
      "ds_read_b128 v[136:139], %[vb] offset:1024\n\t"
      "v_mfma_f32_16x16x32_bf16 v[88:91], v[116:119], v[132:135], v[88:91]\n\t"
      "ds_read_b64 v[114:115], %[va] offset:1544\n\t"
      "v_mfma_f32_16x16x32_bf16 v[84:87], v[116:119], v[128:131], v[84:87]\n\t"
      "ds_read_b64 v[116:117], %[va] offset:1024\n\t"
      "s_waitcnt lgkmcnt(15)\n\t"
      "v_mfma_f32_16x16x32_bf16 v[92:95], v[120:123], v[132:135], v[92:95]\n\t"
      "ds_read_b128 v[132:135], %[vb] offset:512\n\t"
      "v_mfma_f32_16x16x32_bf16 v[88:91], v[120:123], v[128:131], v[88:91]\n\t"
      "ds_read_b64 v[118:119], %[va] offset:1032\n\t"
      "s_waitcnt lgkmcnt(12)\n\t"
      "v_mfma_f32_16x16x32_bf16 v[92:95], v[124:127], v[128:131], v[92:95]\n\t"
      "ds_read_b128 v[128:131], %[vb] offset:0\n\t"
      "v_subrev_u32_e32 %[va], 64, %[va]\n\t"
      "v_subrev_u32_e32 %[vb], 64, %[vb]\n\t"
      "s_sub_u32 s40, s40, 1\n\t"
      "s_cmp_lg_u32 s40, 0\n\t"
      "s_cbranch_scc1 Lc1_%=\n\t"
      "v_add_u32_e32 %[va], 0x240, %[va]\n\t"
      "v_add_u32_e32 %[vb], 0x240, %[vb]\n\t"
      "s_nop 2\n\t"
      "ds_write_b128 %[vy], v[80:83] offset:4096\n\t"
      "ds_write_b128 %[vy], v[84:87] offset:5120\n\t"
      "ds_write_b128 %[vy], v[88:91] offset:6144\n\t"
      "ds_write_b128 %[vy], v[92:95] offset:7168\n\t"
      "s_waitcnt lgkmcnt(0)\n\t"
      "s_barrier\n\t"
        : [va]"+v"(va),[vb]"+v"(vb) : [vy]"v"(lds+O_Y+w*8192+64*li+16*g),[vf]"v"(lds+4*w),[one]"v"(1u) : "memory","scc","s40","v64","v65","v66","v67","v68","v69","v70","v71","v72","v73","v74","v75","v76","v77","v78","v79","v80","v81","v82","v83","v84","v85","v86","v87","v88","v89","v90","v91","v92","v93","v94","v95","v96","v97","v98","v99","v100","v101","v102","v103","v104","v105","v106","v107","v108","v109","v110","v111","v112","v113","v114","v115","v116","v117","v118","v119","v120","v121","v122","v123","v124","v125","v126","v127","v128","v129","v130","v131","v132","v133","v134","v135","v136","v137","v138","v139","v140","v141","v142","v143","v144","v145","v146","v147","v148","v149","v150","v151","v152","v153","v154","v155","v156","v157","v158","v159");
    TS(1); TS(2); TS(3);
  } else {
    const unsigned t=threadIdx.x-256;

    const char* pcb; asm("s_getpc_b64 s[40:41]\n\ts_add_u32 s40, s40, k2_ssm_mix@rel32@lo+4\n\ts_addc_u32 s41, s41, k2_ssm_mix@rel32@hi+12\n\ts_mov_b64 %0, s[40:41]":"=s"(pcb)::"scc","s40","s41");
    const unsigned cw=*(const GLB unsigned*)(pcb+min(t*128u,(unsigned)K2_CODE_BYTES-4u));
    SB();
    u32x4 pv[2][8]; f32x2 gv[4]; float pre[4][8];
    const char* Pb=(const char*)P+((size_t)dg*1024+t)*16;
    auto ld=[&](int i){
#pragma unroll
      for(int s=0;s<8;s++) pv[i&1][s]=*(const u32x4*)(Pb+s*(144*16384)+i*4096);
      SB(); };
#pragma unroll
    for(int i=0;i<4;i++) gv[i]=*(const f32x2*)(G+2*(t+256*i));
    ld(0); ld(1);
    TS(0);
    asm volatile(""::"v"(cw));
    k2_sum(pv[0],gv[0],pre[0]); SB(); ld(2);
    k2_sum(pv[1],gv[1],pre[1]); SB(); ld(3);
    char* op=(char*)out+(size_t)t*(4*D)+8*dg; const char* Y=smem+O_Y+8*t;
    TS(1);
    SB();
    { u32x4 f; unsigned z;
      asm volatile("Lw_%=:\n\tds_read_b128 %0, %2\n\ts_waitcnt lgkmcnt(0)\n\tv_and_b32 %1, v152, v153\n\tv_and_b32 %1, %1, v154\n\tv_and_b32 %1, %1, v155\n\ts_nop 0\n\tv_readfirstlane_b32 s40, %1\n\ts_nop 1\n\t"
                   "s_cmp_lg_u32 s40, 0\n\ts_cbranch_scc1 Ld_%=\n\ts_sleep 1\n\ts_branch Lw_%=\n\tLd_%=:":"={v[152:155]}"(f),"=&v"(z):"v"(lds):"memory","scc","s40"); }
    SB();
    TS(2);
    { f32x2 y[2][4];
#pragma unroll
      for(int i=0;i<2;i++)
#pragma unroll
        for(int c=0;c<4;c++) y[i][c]=*(const f32x2*)(Y+c*8192+2048*i);
      SB();
      k2_fin(y[0],gv[0],pre[0],op); k2_fin(y[1],gv[1],pre[1],op+256*4*D); }
    SB();
    k2_sum(pv[0],gv[2],pre[2]); k2_sum(pv[1],gv[3],pre[3]);
    TS(3);
    BAR();
    TS(4);
    { f32x2 y[2][4];
#pragma unroll
      for(int i=0;i<2;i++)
#pragma unroll
        for(int c=0;c<4;c++) y[i][c]=*(const f32x2*)(Y+c*8192+4096+2048*i);
      SB();
      k2_fin(y[0],gv[2],pre[2],op+512*4*D); k2_fin(y[1],gv[3],pre[3],op+768*4*D); }
  }
}
extern "C" void kernel_launch(void* const* d_in,const int* in_sizes,int n_in,void* d_out,int out_size,void* d_ws,size_t ws_size,hipStream_t stream){
  char* ws=(char*)d_ws;
  u16* P=(u16*)ws; float* G=(float*)(ws+(20<<20)); u16* XT=(u16*)(ws+(21<<20)); char* KR=ws+(24<<20);
  k1_mlp<<<256,512,0,stream>>>((const u16*)d_in[0],(const u16*)d_in[8],(const u16*)d_in[9],(const u16*)d_in[10],(const u16*)d_in[11],
                               (const u16*)d_in[1],(const u16*)d_in[2],(const u16*)d_in[3],(const u16*)d_in[4],(const u16*)d_in[5],(const u16*)d_in[6],P,G,XT,KR
  );
  k2_ssm_mix<<<144,512,0,stream>>>(XT,KR,P,G,(u16*)d_out
  );
}
```
